```python
import torch

def _softmax(x, axis=-1):
    return torch.softmax(x, dim=axis)
def _gelu(x):
    return torch.nn.functional.gelu(x, approximate="tanh")
def _silu(x):
    return torch.nn.functional.silu(x)
def _sigmoid(x):
    return torch.sigmoid(x)
def _layernorm(x, axis=-1, eps=1e-5):
    m = x.mean(dim=axis, keepdim=True)
    v = x.var(dim=axis, keepdim=True, unbiased=False)
    return (x - m) / torch.sqrt(v + eps)
def _scatter_add(tgt, idx, src, axis=0):
    return tgt.clone().index_add_(axis, idx, src)
def _segment_sum(src, seg, n):
    out = torch.zeros((n,) + src.shape[1:], dtype=src.dtype)
    return out.index_add_(0, seg, src)
def _pad(x, pw):
    flat = tuple(v for pair in reversed(pw) for v in pair)
    return torch.nn.functional.pad(x, flat)

kernel_name = 'standalone__deep_cross_xdeepfm__recsys'

def setup_inputs():

    _w = (lambda a: (lambda t: t.to(torch.bfloat16) if t.is_floating_point() else t)(torch.as_tensor(a)))
    B, F, D, L = 1024, 26, 128, 3
    V = min(100000, 100000)
    Din = F * D
    return {
        "idx": _w((lambda lo, hi, *s: torch.randint(lo, hi, s))(0, V, B, F).to(torch.int64)),
        "table": _w((lambda *s: torch.randn(*s, dtype=torch.bfloat16) * 0.8)(V, D)),
        "ws_cin": _w((lambda *s: torch.randn(*s, dtype=torch.bfloat16) * 0.2)(L, F, F, F) * (1.0 / F)),
        "w_cin_out": _w((lambda *s: torch.randn(*s, dtype=torch.bfloat16) * 0.2)(L * F) * ((L * F) ** -0.5)),
        "w_dnn1": _w((lambda *s: torch.randn(*s, dtype=torch.bfloat16) * 0.8)(Din, Din) * (Din ** -0.5)),
        "b_dnn1": _w((lambda *s: torch.randn(*s, dtype=torch.bfloat16) * 3.2)(Din) * 0.1),
        "w_dnn2": _w((lambda *s: torch.randn(*s, dtype=torch.bfloat16) * 0.2)(Din) * (Din ** -0.5)),
        "b_out": _w((lambda *s: torch.randn(*s, dtype=torch.bfloat16) * 0.2)(1)),
    }

def reference(idx, table, ws_cin, w_cin_out, w_dnn1, b_dnn1, w_dnn2, b_out):
    x0 = table[idx]
    B = x0.shape[0]
    L = ws_cin.shape[0]
    xk = x0
    pooled = []
    for l in range(L):
        xk = torch.einsum('bid,bjd,hij->bhd', xk, x0, ws_cin[l])
        pooled.append(xk.sum(axis=-1))
    p_plus = torch.stack(pooled, axis=1).reshape(B, -1)
    logit_cin = p_plus @ w_cin_out
    h = x0.reshape(B, -1)
    h = _gelu(h @ w_dnn1 + b_dnn1)
    logit_dnn = h @ w_dnn2
    return logit_cin + logit_dnn + b_out
```

```cpp
#include <hip/hip_runtime.h>
#include <hip/hip_bf16.h>
typedef unsigned short u16;
__device__ __forceinline__ float bf2f(u16 x){ return __uint_as_float(((unsigned)x)<<16); }
constexpr int B_=1024,F_=26,D_=128,DIN=3328;
__global__ void cin_naive(const int* idx,const u16* table,const u16* wc,const u16* wout,float* logit){
  __shared__ float x0[F_][D_], xk[F_][D_], xn[F_][D_];
  __shared__ float red[D_];
  int b=blockIdx.x,d=threadIdx.x;
  for(int f=0;f<F_;f++){ float v=bf2f(table[(size_t)idx[b*F_+f]*D_+d]); x0[f][d]=v; xk[f][d]=v; }
  float acc=0.f;
  for(int l=0;l<3;l++){
    for(int h=0;h<F_;h++){
      float s=0.f;
      for(int i=0;i<F_;i++){ float t=0.f; for(int j=0;j<F_;j++) t+=bf2f(wc[((l*F_+h)*F_+i)*F_+j])*x0[j][d]; s+=t*xk[i][d]; }
      xn[h][d]=s; acc+=s*bf2f(wout[l*F_+h]);
    }
    for(int h=0;h<F_;h++) xk[h][d]=xn[h][d];
  }
  red[d]=acc; __syncthreads();
  if(d==0){ float s=0; for(int i=0;i<D_;i++) s+=red[i]; logit[b]=s; }
}
__global__ void dnn_naive(const int* idx,const u16* table,const u16* w1,const u16* b1,const u16* w2,float* part){
  __shared__ float a[8][128];
  __shared__ float red[8][256];
  int bg=blockIdx.x, nc=blockIdx.y, t=threadIdx.x; int n=nc*256+t;
  float acc0=0,acc1=0,acc2=0,acc3=0,acc4=0,acc5=0,acc6=0,acc7=0;
  for(int f=0;f<F_;f++){
    __syncthreads();
    for(int e=t;e<8*128;e+=256){ int r=e>>7,d=e&127; a[r][d]=bf2f(table[(size_t)idx[(bg*8+r)*F_+f]*D_+d]); }
    __syncthreads();
    for(int d=0;d<128;d++){ float w=bf2f(w1[(size_t)(f*128+d)*DIN+n]);
      acc0+=a[0][d]*w;acc1+=a[1][d]*w;acc2+=a[2][d]*w;acc3+=a[3][d]*w;acc4+=a[4][d]*w;acc5+=a[5][d]*w;acc6+=a[6][d]*w;acc7+=a[7][d]*w; }
  }
  float bb=bf2f(b1[n]), ww=bf2f(w2[n]);
  auto g=[&](float x){ x+=bb; float u=0.7978845608028654f*(x+0.044715f*x*x*x); return 0.5f*x*(1.f+tanhf(u))*ww; };
  red[0][t]=g(acc0);red[1][t]=g(acc1);red[2][t]=g(acc2);red[3][t]=g(acc3);red[4][t]=g(acc4);red[5][t]=g(acc5);red[6][t]=g(acc6);red[7][t]=g(acc7);
  __syncthreads();
  if(t<8){ float s=0; for(int i=0;i<256;i++) s+=red[t][i]; part[(bg*8+t)*13+nc]=s; }
}
__global__ void fin(const float* logit,const float* part,const u16* bout,__hip_bfloat16* out){
  int b=blockIdx.x*256+threadIdx.x; float s=logit[b]+bf2f(bout[0]); for(int i=0;i<13;i++) s+=part[b*13+i]; out[b]=__float2bfloat16(s);
}
extern "C" void kernel_launch(void* const* d_in,const int* in_sizes,int n_in,void* d_out,int out_size,void* d_ws,size_t ws_size,hipStream_t stream){
  const int* idx=(const int*)d_in[0]; const u16* table=(const u16*)d_in[1]; const u16* wc=(const u16*)d_in[2]; const u16* wout=(const u16*)d_in[3];
  const u16* w1=(const u16*)d_in[4]; const u16* b1=(const u16*)d_in[5]; const u16* w2=(const u16*)d_in[6]; const u16* bout=(const u16*)d_in[7];
  float* logit=(float*)d_ws; float* part=logit+1024;
  cin_naive<<<1024,128,0,stream>>>(idx,table,wc,wout,logit);
  dnn_naive<<<dim3(128,13),256,0,stream>>>(idx,table,w1,b1,w2,part);
  fin<<<4,256,0,stream>>>(logit,part,bout,(__hip_bfloat16*)d_out);
}
```

```cpp
#include <hip/hip_runtime.h>
#include <hip/hip_bf16.h>
typedef unsigned short u16;
typedef int i32x4 __attribute__((ext_vector_type(4)));
typedef unsigned u32x4 __attribute__((ext_vector_type(4)));
typedef short s16x4 __attribute__((ext_vector_type(4)));
typedef short s16x8 __attribute__((ext_vector_type(8)));
typedef __bf16 bf16x8 __attribute__((ext_vector_type(8)));
typedef __bf16 bf16x2 __attribute__((ext_vector_type(2)));
typedef float f32x2 __attribute__((ext_vector_type(2)));
typedef float f32x4 __attribute__((ext_vector_type(4)));
typedef float f32x16 __attribute__((ext_vector_type(16)));
#define DI __device__ __forceinline__
#define PROBE 0
constexpr int NB=1024,NF=26,ND=128,DIN=3328,ROWB=DIN*2;
DI float bf2f(u16 x){ return __uint_as_float(((unsigned)x)<<16); }
DI unsigned cvtpk(float lo,float hi){ f32x2 v={lo,hi}; bf16x2 b=__builtin_convertvector(v,bf16x2); return __builtin_bit_cast(unsigned,b); }
DI void dma(unsigned m0v, unsigned voff, i32x4 srd, unsigned soff){
  asm volatile("s_mov_b32 m0, %0\n\ts_nop 0\n\tbuffer_load_dwordx4 %1, %2, %3 offen lds" :: "s"(m0v), "v"(voff), "s"(srd), "s"(soff) : "memory");
}
DI void dma_nt(unsigned m0v, unsigned voff, i32x4 srd, unsigned soff){
  asm volatile("s_mov_b32 m0, %0\n\ts_nop 0\n\tbuffer_load_dwordx4 %1, %2, %3 offen nt lds" :: "s"(m0v), "v"(voff), "s"(srd), "s"(soff) : "memory");
}
template<int N> DI void waitv(){ asm volatile("s_waitcnt vmcnt(%0)"::"n"(N):"memory"); }
DI i32x4 mksrd(const void* p, unsigned bytes){
  unsigned long long a=(unsigned long long)p; i32x4 s;
  s[0]=__builtin_amdgcn_readfirstlane((unsigned)a); s[1]=__builtin_amdgcn_readfirstlane((unsigned)(a>>32)&0xffff); s[2]=bytes; s[3]=0x20000; return s;
}
__host__ __device__ constexpr int tri_a(int u){ int a=0; while(u>=13-a){ u-=13-a; a++; } return a; }
__host__ __device__ constexpr int tri_b(int u){ int a=0; while(u>=13-a){ u-=13-a; a++; } return a+u; }
__host__ __device__ constexpr int slot_a(int t){ return t<182 ? tri_a(t>>1) : 0; }
__host__ __device__ constexpr int slot_b(int t){ return t<182 ? tri_b(t>>1)+13*(t&1) : 0; }

__global__ __launch_bounds__(64) void k_fold(const u16* wc,const u16* wout,u32x4* frag){
  __shared__ float P3[NF*NF];
  const int q=blockIdx.x, l=threadIdx.x, r=l&31, h=l>>5;
  const u16* W1=wc; const u16* W2=wc+17576; const u16* W3=wc+2*17576;
  const bool needP = q<46 && (q&1);
  if(needP){
    for(int e=l;e<676;e+=64){ float s=0; for(int g=0;g<NF;g++) s+=bf2f(wout[52+g])*bf2f(W3[g*676+e]); P3[e]=s; }
  }
  __syncthreads();
  float v[8];
  #pragma unroll
  for(int j=0;j<8;j++){
    float x=0.f;
    if(r<NF){
      if(q<46){
        int t=(q>>1)*8+j;
        if(t<182){
          int a=slot_a(t), b=slot_b(t);
          bool dup = h==1 && (b==a+13);
          int pa=(a+13*h)%26, pb=(b+13*h)%26;
          if(!dup){
            if(!(q&1)){ x=bf2f(W1[r*676+pa*26+pb]); if(pa!=pb) x+=bf2f(W1[r*676+pb*26+pa]); }
            else { for(int g=0;g<NF;g++){ x+=bf2f(W2[g*676+r*26+pa])*P3[g*26+pb]; if(pa!=pb) x+=bf2f(W2[g*676+r*26+pb])*P3[g*26+pa]; } }
          }
        }
      } else {
        int i=(q-46)*8+j;
        if(i<13){ int p=i+13*h; for(int g=0;g<NF;g++) x+=bf2f(wout[26+g])*bf2f(W2[g*676+r*26+p]); }
      }
    }
    v[j]=x;
  }
  u32x4 o; o[0]=cvtpk(v[0],v[1]); o[1]=cvtpk(v[2],v[3]); o[2]=cvtpk(v[4],v[5]); o[3]=cvtpk(v[6],v[7]);
  frag[q*64+l]=o;
}
template<int S> DI bf16x8 zfrag(const float (&R)[26]){
  u32x4 o;
  #pragma unroll
  for(int j=0;j<4;j++){ constexpr int dummy=0; (void)dummy;
    const int t0=S*8+2*j, t1=t0+1;
    o[j]=cvtpk(R[slot_a(t0)]*R[slot_b(t0)], R[slot_a(t1)]*R[slot_b(t1)]); }
  return __builtin_bit_cast(bf16x8,o);
}
template<int S> struct CinStep {
  static DI void run(const float (&R0)[26],const float (&R1)[26],const bf16x8* wl,int lane,f32x16& x0,f32x16& v0,f32x16& x1,f32x16& v1){
    bf16x8 wa=wl[(2*S)*64+lane], wb=wl[(2*S+1)*64+lane];
    bf16x8 z0=zfrag<S>(R0), z1=zfrag<S>(R1);
    x0=__builtin_amdgcn_mfma_f32_32x32x16_bf16(wa,z0,x0,0,0,0);
    v0=__builtin_amdgcn_mfma_f32_32x32x16_bf16(wb,z0,v0,0,0,0);
    x1=__builtin_amdgcn_mfma_f32_32x32x16_bf16(wa,z1,x1,0,0,0);
    v1=__builtin_amdgcn_mfma_f32_32x32x16_bf16(wb,z1,v1,0,0,0);
    CinStep<S+1>::run(R0,R1,wl,lane,x0,v0,x1,v1);
  }
};
template<> struct CinStep<23> { static DI void run(const float (&)[26],const float (&)[26],const bf16x8*,int,f32x16&,f32x16&,f32x16&,f32x16&){} };

__global__ __launch_bounds__(512) void k_cin(const int* idx,const u16* table,const u16* wout,const u32x4* frag,float* cinp){
  __shared__ u32x4 wl[48*64];
  const int tid=threadIdx.x, lane=tid&63, w=tid>>6, c=lane&31, h=lane>>5;
  for(int e=tid;e<48*64;e+=512) wl[e]=frag[e];
  const int b=blockIdx.x*4+(w>>1);
  float R0[26],R1[26];
  #pragma unroll
  for(int i=0;i<26;i++){ int f=i+13*h; f=f>=26?f-26:f; unsigned v=*(const unsigned*)(table+(size_t)idx[b*NF+f]*ND+(w&1)*64+2*c);
    R0[i]=__uint_as_float(v<<16); R1[i]=__uint_as_float(v&0xffff0000u); }
  f32x16 x0={},x1={},v0,v1;
  #pragma unroll
  for(int g=0;g<16;g++){ int row=(g&3)+8*(g>>2)+4*h; float o=row<NF?bf2f(wout[row]):0.f; v0[g]=o; v1[g]=o; }
  __syncthreads();
  const bf16x8* wlb=(const bf16x8*)wl;
  CinStep<0>::run(R0,R1,wlb,lane,x0,v0,x1,v1);
  { u32x4 z0,z1;
    #pragma unroll
    for(int j=0;j<4;j++){ z0[j]=cvtpk(R0[2*j],R0[2*j+1]); z1[j]=cvtpk(R1[2*j],R1[2*j+1]); }
    bf16x8 wa=wlb[46*64+lane];
    v0=__builtin_amdgcn_mfma_f32_32x32x16_bf16(wa,__builtin_bit_cast(bf16x8,z0),v0,0,0,0);
    v1=__builtin_amdgcn_mfma_f32_32x32x16_bf16(wa,__builtin_bit_cast(bf16x8,z1),v1,0,0,0);
    #pragma unroll
    for(int j=0;j<4;j++){ z0[j]=cvtpk(R0[8+2*j],R0[8+2*j+1]); z1[j]=cvtpk(R1[8+2*j],R1[8+2*j+1]); }
    wa=wlb[47*64+lane];
    v0=__builtin_amdgcn_mfma_f32_32x32x16_bf16(wa,__builtin_bit_cast(bf16x8,z0),v0,0,0,0);
    v1=__builtin_amdgcn_mfma_f32_32x32x16_bf16(wa,__builtin_bit_cast(bf16x8,z1),v1,0,0,0);
  }
  float s=0.f;
  #pragma unroll
  for(int g=0;g<16;g++){ s+=x0[g]*v0[g]; s+=x1[g]*v1[g]; }
  #pragma unroll
  for(int o=32;o>=1;o>>=1) s+=__shfl_xor(s,o);
  if(lane==0) cinp[b*2+(w&1)]=s;
}

DI float gelu_w(float x,float w2){
  float u=x*(0.7978845608028654f+0.0356774081363001f*x*x);
  float e=__builtin_amdgcn_exp2f(-2.885390081777927f*u);
  return x*__builtin_amdgcn_rcpf(1.f+e)*w2;
}
typedef __attribute__((address_space(3))) s16x4* lds_s16x4;
__global__ __launch_bounds__(256) void k_gemm(const int* idx,const u16* table,const u16* w1,const u16* b1,const u16* w2,float* part){
  extern __shared__ __attribute__((aligned(1024))) char lds[];
  constexpr int STG=32768, NS=3;
  const int tid=threadIdx.x, lane=tid&63, w=__builtin_amdgcn_readfirstlane(tid>>6);
  const int wg=blockIdx.x; const int t=(wg&7)*26+(wg>>3); const int mt=t&7, nt=t>>3;
  int* sidx=(int*)(lds+NS*STG);
  { int tmp[13];
    #pragma unroll
    for(int i=0;i<13;i++) tmp[i]=idx[mt*128*26+tid+i*256];
    #pragma unroll
    for(int i=0;i<13;i++) sidx[tid+i*256]=tmp[i]*256; }
  __syncthreads();
  i32x4 srdT=mksrd(table,100000u*256u), srdW=mksrd(w1,(unsigned)DIN*ROWB);
  unsigned wv[4]; unsigned asw[4];
  #pragma unroll
  for(int q=0;q<4;q++){
    int rho=16*w+4*q+(lane>>4);
    int r32=rho&31, tt=r32>>4, g=(r32>>2)&3, qq=r32&3; int k=(rho&32)+8*g+4*tt+qq;
    int ch=(lane&15)^((rho&7)<<1);
    wv[q]=k*ROWB+nt*256+ch*16;
    int row=32*w+8*q+(lane>>3); asw[q]=((lane&7)^((row>>1)&7))*16;
  }
  auto stage=[&](int s){ const int f=s>>1; unsigned base=(s%NS)*STG;
    #pragma unroll
    for(int q=0;q<4;q++){ int row=32*w+8*q+(lane>>3); unsigned a=sidx[row*26+f]+asw[q]; dma(base+(4*w+q)*1024,a,srdT,(s&1)*128); }
    #pragma unroll
    for(int q=0;q<4;q++) dma(base+16384+(4*w+q)*1024,wv[q],srdW,s*64*ROWB);
  };
  f32x4 acc[4][4]={};
  const int wm=w>>1, wn=w&1;
  unsigned aoff[2];
  { int row=wm*64+(lane&15); for(int kk=0;kk<2;kk++) aoff[kk]=row*128+(((4*kk+(lane>>4))^((row>>1)&7))*16); }
  const int trrow=4*(lane>>4)+((lane&15)>>2);
  stage(0); stage(1);
  for(int s=0;s<52;s++){
    if(s<51) waitv<8>(); else waitv<0>();
    asm volatile("s_waitcnt lgkmcnt(0)\n\ts_barrier":::"memory");
    if(s+2<52) stage(s+2);
    const char* base=lds+(s%NS)*STG;
    #pragma unroll
    for(int kk=0;kk<2;kk++){
      bf16x8 a[4], bfr[4];
      #pragma unroll
      for(int m=0;m<4;m++) a[m]=*(const bf16x8*)(base+aoff[kk]+m*2048);
      #pragma unroll
      for(int n=0;n<4;n++){
        int nb=wn*4+n;
        unsigned o=16384+(32*kk+trrow)*256+((nb^(trrow&7))*32)+(lane&3)*8;
        s16x4 lo=__builtin_amdgcn_ds_read_tr16_b64_v4i16((lds_s16x4)(base+o));
        s16x4 hi=__builtin_amdgcn_ds_read_tr16_b64_v4i16((lds_s16x4)(base+o+16*256));
        bfr[n]=__builtin_bit_cast(bf16x8,__builtin_shufflevector(lo,hi,0,1,2,3,4,5,6,7));
      }
      #pragma unroll
      for(int m=0;m<4;m++)
        #pragma unroll
        for(int n=0;n<4;n++) acc[m][n]=__builtin_amdgcn_mfma_f32_16x16x32_bf16(a[m],bfr[n],acc[m][n],0,0,0);
    }
  }
  float rs[4][4];
  #pragma unroll
  for(int m=0;m<4;m++)
    #pragma unroll
    for(int j=0;j<4;j++) rs[m][j]=0.f;
  #pragma unroll
  for(int n=0;n<4;n++){ int col=nt*128+wn*64+n*16+(lane&15); float bb=bf2f(b1[col]), ww=bf2f(w2[col]);
    #pragma unroll
    for(int m=0;m<4;m++)
      #pragma unroll
      for(int j=0;j<4;j++) rs[m][j]+=gelu_w(acc[m][n][j]+bb,ww);
  }
  #pragma unroll
  for(int m=0;m<4;m++)
    #pragma unroll
    for(int j=0;j<4;j++){ float s=rs[m][j]; s+=__shfl_xor(s,1); s+=__shfl_xor(s,2); s+=__shfl_xor(s,4); s+=__shfl_xor(s,8);
      if((lane&15)==0) part[(mt*128+wm*64+m*16+4*(lane>>4)+j)*52+nt*2+wn]=s; }
}
__global__ void k_final(const float* cinp,const float* part,const u16* bout,__hip_bfloat16* out){
  int b=blockIdx.x*64+threadIdx.x; float s=cinp[2*b]+cinp[2*b+1]+bf2f(bout[0]);
  const f32x4* p=(const f32x4*)(part+b*52);
  #pragma unroll
  for(int i=0;i<13;i++){ f32x4 v=p[i]; s+=v[0]+v[1]+v[2]+v[3]; }
  out[b]=__float2bfloat16(s);
}
template<int NT> __global__ __launch_bounds__(256) void k_probe(const u16* w1,int n){
  extern __shared__ __attribute__((aligned(1024))) char lds[];
  const int tid=threadIdx.x, lane=tid&63, w=__builtin_amdgcn_readfirstlane(tid>>6);
  i32x4 srdW=mksrd(w1,(unsigned)DIN*ROWB);
  for(int p=blockIdx.x*4+w; p<n; p+=1024){ if(NT) dma_nt(w*1024,lane*16,srdW,p*1024); else dma(w*1024,lane*16,srdW,p*1024); }
  waitv<0>();
}
extern "C" void kernel_launch(void* const* d_in,const int* in_sizes,int n_in,void* d_out,int out_size,void* d_ws,size_t ws_size,hipStream_t stream){
  const int* idx=(const int*)d_in[0]; const u16* table=(const u16*)d_in[1]; const u16* wc=(const u16*)d_in[2]; const u16* wout=(const u16*)d_in[3];
  const u16* w1=(const u16*)d_in[4]; const u16* b1=(const u16*)d_in[5]; const u16* w2=(const u16*)d_in[6]; const u16* bout=(const u16*)d_in[7];
  u32x4* frag=(u32x4*)d_ws; float* cinp=(float*)((char*)d_ws+65536); float* part=(float*)((char*)d_ws+131072);
  constexpr int GL=3*32768+128*26*4;
  hipFuncSetAttribute((const void*)k_gemm,hipFuncAttributeMaxDynamicSharedMemorySize,GL);
  if(PROBE==1) k_probe<0><<<256,256,4096,stream>>>(w1,21632);
  if(PROBE==2) k_probe<1><<<256,256,4096,stream>>>(w1,21632);
  if(PROBE==3) k_probe<0><<<256,256,4096,stream>>>(w1,0);
  k_fold<<<48,64,0,stream>>>(wc,wout,frag);
  k_gemm<<<208,256,GL,stream>>>(idx,table,w1,b1,w2,part);
  k_cin<<<256,512,0,stream>>>(idx,table,wout,frag,cinp);
  k_final<<<16,64,0,stream>>>(cinp,part,bout,(__hip_bfloat16*)d_out);
}
```

```cpp
#include <hip/hip_runtime.h>
#include <hip/hip_bf16.h>
typedef unsigned short u16;
typedef int i32x4 __attribute__((ext_vector_type(4)));
typedef unsigned u32x4 __attribute__((ext_vector_type(4)));
typedef short s16x4 __attribute__((ext_vector_type(4)));
typedef short s16x8 __attribute__((ext_vector_type(8)));
typedef __bf16 bf16x8 __attribute__((ext_vector_type(8)));
typedef __bf16 bf16x2 __attribute__((ext_vector_type(2)));
typedef float f32x2 __attribute__((ext_vector_type(2)));
typedef float f32x4 __attribute__((ext_vector_type(4)));
typedef float f32x16 __attribute__((ext_vector_type(16)));
#define DI __device__ __forceinline__
#define PROBE 0
constexpr int NB=1024,NF=26,ND=128,DIN=3328,ROWB=DIN*2;
DI float bf2f(u16 x){ return __uint_as_float(((unsigned)x)<<16); }
DI unsigned cvtpk(float lo,float hi){ f32x2 v={lo,hi}; bf16x2 b=__builtin_convertvector(v,bf16x2); return __builtin_bit_cast(unsigned,b); }
DI void dma(unsigned m0v, unsigned voff, i32x4 srd, unsigned soff){
  asm volatile("s_mov_b32 m0, %0\n\ts_nop 0\n\tbuffer_load_dwordx4 %1, %2, %3 offen lds" :: "s"(m0v), "v"(voff), "s"(srd), "s"(soff) : "memory");
}
DI void dma_nt(unsigned m0v, unsigned voff, i32x4 srd, unsigned soff){
  asm volatile("s_mov_b32 m0, %0\n\ts_nop 0\n\tbuffer_load_dwordx4 %1, %2, %3 offen nt lds" :: "s"(m0v), "v"(voff), "s"(srd), "s"(soff) : "memory");
}
template<int N> DI void waitv(){ asm volatile("s_waitcnt vmcnt(%0)"::"n"(N):"memory"); }
DI i32x4 mksrd(const void* p, unsigned bytes){
  unsigned long long a=(unsigned long long)p; i32x4 s;
  s[0]=__builtin_amdgcn_readfirstlane((unsigned)a); s[1]=__builtin_amdgcn_readfirstlane((unsigned)(a>>32)&0xffff); s[2]=bytes; s[3]=0x20000; return s;
}
__host__ __device__ constexpr int tri_a(int u){ int a=0; while(u>=13-a){ u-=13-a; a++; } return a; }
__host__ __device__ constexpr int tri_b(int u){ int a=0; while(u>=13-a){ u-=13-a; a++; } return a+u; }
__host__ __device__ constexpr int slot_a(int t){ return t<182 ? tri_a(t>>1) : 0; }
__host__ __device__ constexpr int slot_b(int t){ return t<182 ? tri_b(t>>1)+13*(t&1) : 0; }

__global__ __launch_bounds__(512) void k_fold(const u16* wc,const u16* wout,u16* frag){
  __shared__ float P3[NF*NF];
  __shared__ __attribute__((aligned(16))) u16 sw[3*17576];
  __shared__ float so[78];
  const int q=blockIdx.x, tid=threadIdx.x, l=tid>>3, j=tid&7, r=l&31, h=l>>5;
  { u32x4 t[13];
    #pragma unroll
    for(int i=0;i<13;i++){ int e=tid+i*512; if(e<6591) t[i]=((const u32x4*)wc)[e]; }
    #pragma unroll
    for(int i=0;i<13;i++){ int e=tid+i*512; if(e<6591) ((u32x4*)sw)[e]=t[i]; } }
  if(tid<78) so[tid]=bf2f(wout[tid]);
  __syncthreads();
  const u16* W1=sw; const u16* W2=sw+17576; const u16* W3=sw+2*17576;
  for(int e=tid;e<676;e+=512){ float s=0; for(int g=0;g<NF;g++) s+=so[52+g]*bf2f(W3[g*676+e]); P3[e]=s; }
  __syncthreads();
  float x=0.f;
  if(r<NF){
    if(q<46){
      int t=(q>>1)*8+j;
      if(t<182){
        int a=slot_a(t), b=slot_b(t);
        bool dup = h==1 && (b==a+13);
        int pa=(a+13*h)%26, pb=(b+13*h)%26;
        if(!dup){
          if(!(q&1)){ x=bf2f(W1[r*676+pa*26+pb]); if(pa!=pb) x+=bf2f(W1[r*676+pb*26+pa]); }
          else { for(int g=0;g<NF;g++){ x+=bf2f(W2[g*676+r*26+pa])*P3[g*26+pb]; if(pa!=pb) x+=bf2f(W2[g*676+r*26+pb])*P3[g*26+pa]; } }
        }
      }
    } else {
      int i=(q-46)*8+j;
      if(i<13){ int p=i+13*h; for(int g=0;g<NF;g++) x+=so[26+g]*bf2f(W2[g*676+r*26+p]); }
    }
  }
  frag[q*512+tid]=(u16)(cvtpk(x,0.f)&0xffff);
}
template<int S> DI bf16x8 zfrag(const float (&R)[26]){
  u32x4 o;
  #pragma unroll
  for(int j=0;j<4;j++){ constexpr int dummy=0; (void)dummy;
    const int t0=S*8+2*j, t1=t0+1;
    o[j]=cvtpk(R[slot_a(t0)]*R[slot_b(t0)], R[slot_a(t1)]*R[slot_b(t1)]); }
  return __builtin_bit_cast(bf16x8,o);
}
template<int S> struct CinStep {
  static DI void run(const float (&R0)[26],const float (&R1)[26],const bf16x8* wl,int lane,f32x16& x0,f32x16& v0,f32x16& x1,f32x16& v1){
    bf16x8 wa=wl[(2*S)*64+lane], wb=wl[(2*S+1)*64+lane];
    bf16x8 z0=zfrag<S>(R0), z1=zfrag<S>(R1);
    x0=__builtin_amdgcn_mfma_f32_32x32x16_bf16(wa,z0,x0,0,0,0);
    v0=__builtin_amdgcn_mfma_f32_32x32x16_bf16(wb,z0,v0,0,0,0);
    x1=__builtin_amdgcn_mfma_f32_32x32x16_bf16(wa,z1,x1,0,0,0);
    v1=__builtin_amdgcn_mfma_f32_32x32x16_bf16(wb,z1,v1,0,0,0);
    CinStep<S+1>::run(R0,R1,wl,lane,x0,v0,x1,v1);
  }
};
template<> struct CinStep<23> { static DI void run(const float (&)[26],const float (&)[26],const bf16x8*,int,f32x16&,f32x16&,f32x16&,f32x16&){} };

__global__ __launch_bounds__(512) void k_cin(const int* idx,const u16* table,const u16* wout,const u32x4* frag,float* cinp){
  __shared__ u32x4 wl[48*64];
  const int tid=threadIdx.x, lane=tid&63, w=tid>>6, c=lane&31, h=lane>>5;
  for(int e=tid;e<48*64;e+=512) wl[e]=frag[e];
  const int b=blockIdx.x*4+(w>>1);
  float R0[26],R1[26];
  #pragma unroll
  for(int i=0;i<26;i++){ int f=i+13*h; f=f>=26?f-26:f; unsigned v=*(const unsigned*)(table+(size_t)idx[b*NF+f]*ND+(w&1)*64+2*c);
    R0[i]=__uint_as_float(v<<16); R1[i]=__uint_as_float(v&0xffff0000u); }
  f32x16 x0={},x1={},v0,v1;
  #pragma unroll
  for(int g=0;g<16;g++){ int row=(g&3)+8*(g>>2)+4*h; float o=row<NF?bf2f(wout[row]):0.f; v0[g]=o; v1[g]=o; }
  __syncthreads();
  const bf16x8* wlb=(const bf16x8*)wl;
  CinStep<0>::run(R0,R1,wlb,lane,x0,v0,x1,v1);
  { u32x4 z0,z1;
    #pragma unroll
    for(int j=0;j<4;j++){ z0[j]=cvtpk(R0[2*j],R0[2*j+1]); z1[j]=cvtpk(R1[2*j],R1[2*j+1]); }
    bf16x8 wa=wlb[46*64+lane];
    v0=__builtin_amdgcn_mfma_f32_32x32x16_bf16(wa,__builtin_bit_cast(bf16x8,z0),v0,0,0,0);
    v1=__builtin_amdgcn_mfma_f32_32x32x16_bf16(wa,__builtin_bit_cast(bf16x8,z1),v1,0,0,0);
    #pragma unroll
    for(int j=0;j<4;j++){ z0[j]=cvtpk(R0[8+2*j],R0[8+2*j+1]); z1[j]=cvtpk(R1[8+2*j],R1[8+2*j+1]); }
    wa=wlb[47*64+lane];
    v0=__builtin_amdgcn_mfma_f32_32x32x16_bf16(wa,__builtin_bit_cast(bf16x8,z0),v0,0,0,0);
    v1=__builtin_amdgcn_mfma_f32_32x32x16_bf16(wa,__builtin_bit_cast(bf16x8,z1),v1,0,0,0);
  }
  float s=0.f;
  #pragma unroll
  for(int g=0;g<16;g++){ s+=x0[g]*v0[g]; s+=x1[g]*v1[g]; }
  #pragma unroll
  for(int o=32;o>=1;o>>=1) s+=__shfl_xor(s,o);
  if(lane==0) cinp[b*2+(w&1)]=s;
}

DI float gelu_w(float x,float w2){
  float u=x*(0.7978845608028654f+0.0356774081363001f*x*x);
  float e=__builtin_amdgcn_exp2f(-2.885390081777927f*u);
  return x*__builtin_amdgcn_rcpf(1.f+e)*w2;
}
typedef __attribute__((address_space(3))) s16x4* lds_s16x4;
__global__ __launch_bounds__(256) void k_gemm(const int* idx,const u16* table,const u16* w1,const u16* b1,const u16* w2,float* part){
  extern __shared__ __attribute__((aligned(1024))) char lds[];
  constexpr int STG=32768, NS=3;
  const int tid=threadIdx.x, lane=tid&63, w=__builtin_amdgcn_readfirstlane(tid>>6);
  const int wg=blockIdx.x; const int t=(wg&7)*26+(wg>>3); const int mt=t&7, nt=t>>3;
  int* sidx=(int*)(lds+NS*STG);
  { int tmp[13];
    #pragma unroll
    for(int i=0;i<13;i++) tmp[i]=idx[mt*128*26+tid+i*256];
    #pragma unroll
    for(int i=0;i<13;i++) sidx[tid+i*256]=tmp[i]*256; }
  __syncthreads();
  i32x4 srdT=mksrd(table,100000u*256u), srdW=mksrd(w1,(unsigned)DIN*ROWB);
  unsigned wv[4]; unsigned asw[4];
  #pragma unroll
  for(int q=0;q<4;q++){
    int rho=16*w+4*q+(lane>>4);
    int r32=rho&31, tt=r32>>4, g=(r32>>2)&3, qq=r32&3; int k=(rho&32)+8*g+4*tt+qq;
    int ch=(lane&15)^((rho&7)<<1);
    wv[q]=k*ROWB+nt*256+ch*16;
    int row=32*w+8*q+(lane>>3); asw[q]=((lane&7)^((row>>1)&7))*16;
  }
  auto stage=[&](int s){ const int f=s>>1; unsigned base=(s%NS)*STG;
    #pragma unroll
    for(int q=0;q<4;q++){ int row=32*w+8*q+(lane>>3); unsigned a=sidx[row*26+f]+asw[q]; dma(base+(4*w+q)*1024,a,srdT,(s&1)*128); }
    #pragma unroll
    for(int q=0;q<4;q++) dma(base+16384+(4*w+q)*1024,wv[q],srdW,s*64*ROWB);
  };
  f32x4 acc[4][4]={};
  const int wm=w>>1, wn=w&1;
  unsigned aoff[2];
  { int row=wm*64+(lane&15); for(int kk=0;kk<2;kk++) aoff[kk]=row*128+(((4*kk+(lane>>4))^((row>>1)&7))*16); }
  const int trrow=4*(lane>>4)+((lane&15)>>2);
  stage(0); stage(1);
  for(int s=0;s<52;s++){
    if(s<51) waitv<8>(); else waitv<0>();
    asm volatile("s_waitcnt lgkmcnt(0)\n\ts_barrier":::"memory");
    if(s+2<52) stage(s+2);
    const char* base=lds+(s%NS)*STG;
    #pragma unroll
    for(int kk=0;kk<2;kk++){
      bf16x8 a[4], bfr[4];
      #pragma unroll
      for(int m=0;m<4;m++) a[m]=*(const bf16x8*)(base+aoff[kk]+m*2048);
      #pragma unroll
      for(int n=0;n<4;n++){
        int nb=wn*4+n;
        unsigned o=16384+(32*kk+trrow)*256+((nb^(trrow&7))*32)+(lane&3)*8;
        s16x4 lo=__builtin_amdgcn_ds_read_tr16_b64_v4i16((lds_s16x4)(base+o));
        s16x4 hi=__builtin_amdgcn_ds_read_tr16_b64_v4i16((lds_s16x4)(base+o+16*256));
        bfr[n]=__builtin_bit_cast(bf16x8,__builtin_shufflevector(lo,hi,0,1,2,3,4,5,6,7));
      }
      #pragma unroll
      for(int m=0;m<4;m++)
        #pragma unroll
        for(int n=0;n<4;n++) acc[m][n]=__builtin_amdgcn_mfma_f32_16x16x32_bf16(a[m],bfr[n],acc[m][n],0,0,0);
    }
  }
  float rs[4][4];
  #pragma unroll
  for(int m=0;m<4;m++)
    #pragma unroll
    for(int j=0;j<4;j++) rs[m][j]=0.f;
  #pragma unroll
  for(int n=0;n<4;n++){ int col=nt*128+wn*64+n*16+(lane&15); float bb=bf2f(b1[col]), ww=bf2f(w2[col]);
    #pragma unroll
    for(int m=0;m<4;m++)
      #pragma unroll
      for(int j=0;j<4;j++) rs[m][j]+=gelu_w(acc[m][n][j]+bb,ww);
  }
  #pragma unroll
  for(int m=0;m<4;m++)
    #pragma unroll
    for(int j=0;j<4;j++){ float s=rs[m][j]; s+=__shfl_xor(s,1); s+=__shfl_xor(s,2); s+=__shfl_xor(s,4); s+=__shfl_xor(s,8);
      if((lane&15)==0) part[(mt*128+wm*64+m*16+4*(lane>>4)+j)*52+nt*2+wn]=s; }
}
__global__ void k_final(const float* cinp,const float* part,const u16* bout,__hip_bfloat16* out){
  int b=blockIdx.x*64+threadIdx.x; float s=cinp[2*b]+cinp[2*b+1]+bf2f(bout[0]);
  const f32x4* p=(const f32x4*)(part+b*52);
  #pragma unroll
  for(int i=0;i<13;i++){ f32x4 v=p[i]; s+=v[0]+v[1]+v[2]+v[3]; }
  out[b]=__float2bfloat16(s);
}
template<int NT> __global__ __launch_bounds__(256) void k_probe(const u16* w1,int n){
  extern __shared__ __attribute__((aligned(1024))) char lds[];
  const int tid=threadIdx.x, lane=tid&63, w=__builtin_amdgcn_readfirstlane(tid>>6);
  i32x4 srdW=mksrd(w1,(unsigned)DIN*ROWB);
  for(int p=blockIdx.x*4+w; p<n; p+=1024){ if(NT) dma_nt(w*1024,lane*16,srdW,p*1024); else dma(w*1024,lane*16,srdW,p*1024); }
  waitv<0>();
}
extern "C" void kernel_launch(void* const* d_in,const int* in_sizes,int n_in,void* d_out,int out_size,void* d_ws,size_t ws_size,hipStream_t stream){
  const int* idx=(const int*)d_in[0]; const u16* table=(const u16*)d_in[1]; const u16* wc=(const u16*)d_in[2]; const u16* wout=(const u16*)d_in[3];
  const u16* w1=(const u16*)d_in[4]; const u16* b1=(const u16*)d_in[5]; const u16* w2=(const u16*)d_in[6]; const u16* bout=(const u16*)d_in[7];
  u32x4* frag=(u32x4*)d_ws; float* cinp=(float*)((char*)d_ws+65536); float* part=(float*)((char*)d_ws+131072);
  constexpr int GL=3*32768+128*26*4;
  (void)hipFuncSetAttribute((const void*)k_gemm,hipFuncAttributeMaxDynamicSharedMemorySize,GL);
  k_fold<<<48,512,0,stream>>>(wc,wout,(u16*)frag);
  k_gemm<<<208,256,GL,stream>>>(idx,table,w1,b1,w2,part);
  k_cin<<<256,512,0,stream>>>(idx,table,wout,frag,cinp);
  k_final<<<16,64,0,stream>>>(cinp,part,bout,(__hip_bfloat16*)d_out);
  if(PROBE==1) k_probe<0><<<256,256,4096,stream>>>(table,21632);
  if(PROBE==2) k_probe<1><<<256,256,4096,stream>>>(table,21632);
  if(PROBE==3) k_probe<0><<<256,256,4096,stream>>>(table,0);
}
```

```cpp
#include <hip/hip_runtime.h>
#include <hip/hip_bf16.h>
typedef unsigned short u16;
typedef unsigned long long u64;
typedef int i32x4 __attribute__((ext_vector_type(4)));
typedef unsigned u32x4 __attribute__((ext_vector_type(4)));
typedef unsigned u32x2 __attribute__((ext_vector_type(2)));
typedef short s16x4 __attribute__((ext_vector_type(4)));
typedef __bf16 bf16x8 __attribute__((ext_vector_type(8)));
typedef __bf16 bf16x2 __attribute__((ext_vector_type(2)));
typedef float f32x2 __attribute__((ext_vector_type(2)));
typedef float f32x4 __attribute__((ext_vector_type(4)));
typedef float f32x16 __attribute__((ext_vector_type(16)));
#define DI __device__ __forceinline__
#define LAS __attribute__((address_space(3)))
#define GAS __attribute__((address_space(1)))
#define STAMPS 0
#define PFD 0
constexpr int NB=1024,NF=26,ND=128,DIN=3328,ROWB=DIN*2;
constexpr int STG=43008, WOFF=16384, WPITCH=416, NSTG=26;
constexpr int L_X0=129024, L_IDX=155648, L_MISC=162816, L_TOTAL=163840;
constexpr int L_CINSUM=L_MISC, L_QUEUE=L_MISC+64, L_PFD=L_MISC+768;
constexpr int L_B6=STG;
constexpr unsigned W_EPOCH=0, W_STARTED=256, W_FFLAG=2048, W_XFLAG=4096, W_FRAG=4096+65536, W_GRAN=W_FRAG+49152, W_XBUF=W_GRAN+262144, W_END=W_XBUF+256*4*7168;
constexpr unsigned SPIN_CAP=1u<<18;

DI float bf2f(unsigned x){ return __uint_as_float(x<<16); }
DI float bflo(unsigned x){ return __uint_as_float(x<<16); }
DI float bfhi(unsigned x){ return __uint_as_float(x&0xffff0000u); }
DI unsigned cvtpk(float lo,float hi){ f32x2 v={lo,hi}; bf16x2 b=__builtin_convertvector(v,bf16x2); return __builtin_bit_cast(unsigned,b); }
DI void dma(unsigned m0v, unsigned voff, i32x4 srd, unsigned soff){
  asm volatile("s_mov_b32 m0, %0\n\ts_nop 0\n\tbuffer_load_dwordx4 %1, %2, %3 offen lds" :: "s"(m0v), "v"(voff), "s"(srd), "s"(soff) : "memory");
}
DI void dmaW8(unsigned m0v, unsigned voff, i32x4 srd, unsigned so){
  unsigned t;
  asm volatile(
    "s_mov_b64 exec, %5\n\t"
    "s_mov_b32 m0, %1\n\ts_nop 0\n\tbuffer_load_dwordx4 %2, %3, %4 offen lds\n\t"
    "s_add_u32 m0, %1, 832\n\ts_add_u32 %0, %4, 13312\n\tbuffer_load_dwordx4 %2, %3, %0 offen lds\n\t"
    "s_add_u32 m0, %1, 1664\n\ts_add_u32 %0, %4, 53248\n\tbuffer_load_dwordx4 %2, %3, %0 offen lds\n\t"
    "s_add_u32 m0, %1, 2496\n\ts_add_u32 %0, %4, 66560\n\tbuffer_load_dwordx4 %2, %3, %0 offen lds\n\t"
    "s_add_u32 m0, %1, 3328\n\ts_add_u32 %0, %4, 106496\n\tbuffer_load_dwordx4 %2, %3, %0 offen lds\n\t"
    "s_add_u32 m0, %1, 4160\n\ts_add_u32 %0, %4, 119808\n\tbuffer_load_dwordx4 %2, %3, %0 offen lds\n\t"
    "s_add_u32 m0, %1, 4992\n\ts_add_u32 %0, %4, 159744\n\tbuffer_load_dwordx4 %2, %3, %0 offen lds\n\t"
    "s_add_u32 m0, %1, 5824\n\ts_add_u32 %0, %4, 173056\n\tbuffer_load_dwordx4 %2, %3, %0 offen lds\n\t"
    "s_mov_b64 exec, -1"
    : "=&s"(t) : "s"(m0v), "v"(voff), "s"(srd), "s"(so), "s"(0x000FFFFFFFFFFFFFull) : "memory","scc");
}
DI void dma_lo32(unsigned m0v, unsigned voff, i32x4 srd, unsigned soff){
  asm volatile("s_mov_b64 exec, %4\n\ts_mov_b32 m0, %0\n\ts_nop 0\n\tbuffer_load_dwordx4 %1, %2, %3 offen lds\n\ts_mov_b64 exec, -1" :: "s"(m0v), "v"(voff), "s"(srd), "s"(soff), "s"(0xFFFFFFFFull) : "memory");
}
DI void pfload(unsigned voff, i32x4 srd, unsigned soff){
  asm volatile("s_mov_b32 m0, %3\n\ts_nop 0\n\tbuffer_load_dword %0, %1, %2 offen lds" :: "v"(voff), "s"(srd), "s"(soff), "n"(L_PFD) : "memory"); }
template<int N> DI void waitv(){ asm volatile("s_waitcnt vmcnt(%0)"::"n"(N):"memory"); }
DI void bar(){ asm volatile("s_barrier":::"memory"); }
DI void barl(){ asm volatile("s_waitcnt lgkmcnt(0)\n\ts_barrier":::"memory"); }
DI i32x4 mksrd(const void* p, unsigned bytes){
  u64 a=(u64)p; i32x4 s;
  s[0]=__builtin_amdgcn_readfirstlane((unsigned)a); s[1]=__builtin_amdgcn_readfirstlane((unsigned)(a>>32)&0xffff); s[2]=bytes; s[3]=0x20000; return s;
}
typedef __amdgpu_buffer_rsrc_t rsrc_t;
DI rsrc_t mkrsrc(const void* p, unsigned bytes){ return __builtin_amdgcn_make_buffer_rsrc((void*)p,0,bytes,0x20000); }
#define SC1 16
#define RLX_AGENT __ATOMIC_RELAXED, __HIP_MEMORY_SCOPE_AGENT
#define SB0() __builtin_amdgcn_sched_barrier(0)
#define STAMP(i)
template<class T> DI T ldsr(unsigned a){ return *(const LAS T*)(size_t)a; }
template<class T> DI void ldsw(unsigned a,T v){ *(LAS T*)(size_t)a=v; }
DI bf16x8 ldB(unsigned a){
  s16x4 lo=__builtin_amdgcn_ds_read_tr16_b64_v4i16((LAS s16x4*)(size_t)a);
  s16x4 hi=__builtin_amdgcn_ds_read_tr16_b64_v4i16((LAS s16x4*)(size_t)(a+16*WPITCH));
  return __builtin_bit_cast(bf16x8,__builtin_shufflevector(lo,hi,0,1,2,3,4,5,6,7));
}
#define MFMA16(a,b,c) __builtin_amdgcn_mfma_f32_16x16x32_bf16(a,b,c,0,0,0)
#define MFMA32(a,b,c) __builtin_amdgcn_mfma_f32_32x32x16_bf16(a,b,c,0,0,0)
template<int CTRL> DI float dppadd(float v){ return v+__int_as_float(__builtin_amdgcn_update_dpp(0,__float_as_int(v),CTRL,0xf,0xf,true)); }
DI float sum16(float v){ v=dppadd<0xB1>(v); v=dppadd<0x4E>(v); v=dppadd<0x141>(v); v=dppadd<0x140>(v); return v; }
DI float sum64(float v){ v=sum16(v); v+=__shfl_xor(v,16); v+=__shfl_xor(v,32); return v; }

template<int T> struct Slot { static constexpr int u=T>>1, a=T<182?u%13:0, b=T<182?((u%13+u/13)%13+13*(T&1)):0; };
constexpr unsigned wfrag_addr(int q){ return q<42? 2*STG+q*1024 : L_IDX+(q-42)*1024; }
template<int S> DI bf16x8 zfrag(const float (&R)[26]){
  u32x4 o;
  o[0]=cvtpk(R[Slot<8*S+0>::a]*R[Slot<8*S+0>::b],R[Slot<8*S+1>::a]*R[Slot<8*S+1>::b]);
  o[1]=cvtpk(R[Slot<8*S+2>::a]*R[Slot<8*S+2>::b],R[Slot<8*S+3>::a]*R[Slot<8*S+3>::b]);
  o[2]=cvtpk(R[Slot<8*S+4>::a]*R[Slot<8*S+4>::b],R[Slot<8*S+5>::a]*R[Slot<8*S+5>::b]);
  o[3]=cvtpk(R[Slot<8*S+6>::a]*R[Slot<8*S+6>::b],R[Slot<8*S+7>::a]*R[Slot<8*S+7>::b]);
  return __builtin_bit_cast(bf16x8,o);
}
template<int S> DI void cin_steps(const float (&R)[26],unsigned l16,f32x16& x,f32x16& v){
  if constexpr(S<23){
    bf16x8 wa=ldsr<bf16x8>(l16+wfrag_addr(2*S)), wb=ldsr<bf16x8>(l16+wfrag_addr(2*S+1));
    bf16x8 z=zfrag<S>(R);
    x=MFMA32(wa,z,x); v=MFMA32(wb,z,v);
    cin_steps<S+1>(R,l16,x,v);
  }
}
DI float gelu_w(float x,float w2){
  float t=x*(-2.302208198f-0.1029432397f*x*x);
  float e=__builtin_amdgcn_exp2f(t);
  return x*__builtin_amdgcn_rcpf(1.f+e)*w2;
}

__global__ __launch_bounds__(512,2) void k_main(const int* __restrict__ idx,const u16* __restrict__ table,const u16* __restrict__ wc,const u16* __restrict__ wout,
    const u16* __restrict__ w1,const u16* __restrict__ b1,const u16* __restrict__ w2,const u16* __restrict__ bout,u16* __restrict__ out,char* ws){
  extern __shared__ __attribute__((aligned(1024))) char lds[];
  const int tid=threadIdx.x, lane=tid&63, w=__builtin_amdgcn_readfirstlane(tid>>6);
  const int wg=blockIdx.x, xcd=wg&7, j=wg>>3, ks=xcd&1, mt=j&7, nl=j>>3, nt=(xcd>>1)*4+nl;
  STAMP(0);
  asm volatile(""::"s"(idx),"s"(table),"s"(wc),"s"(wout),"s"(w1),"s"(b1),"s"(w2),"s"(bout),"s"(out),"s"(ws));
  unsigned ep;
  auto load_ep=[&](){ asm volatile("s_load_dword %0, %1, 0x0\n\ts_waitcnt lgkmcnt(0)":"=s"(ep):"s"(ws):"memory"); ep+=1u; };
  const rsrc_t rsW=mkrsrc(ws,W_END);
  f32x4 keep[2][7];
  const int wm=(w>>1)&1, wn=w&1;
  if(w>=4){
    const int q=w-4;
    i32x4 srdT=mksrd(table,100000u*256u), srdW=mksrd(w1,(unsigned)DIN*ROWB), srdI=mksrd(idx,NB*NF*4);
    const int r8=lane>>3, c8=lane&7;
    u32x2 iv[4]; unsigned ix;
    #pragma unroll
    for(int p=0;p<4;p++){ unsigned o=((mt*128+32*q+8*p+r8)*NF+ks*13+2*c8)*4;
      asm volatile("buffer_load_dwordx2 %0, %1, %2, 0 offen":"=v"(iv[p]):"v"(o),"s"(srdI):"memory"); }
    { unsigned o=((wg*4+q)*NF+(lane<26?lane:0))*4; asm volatile("buffer_load_dword %0, %1, %2, 0 offen":"=v"(ix):"v"(o),"s"(srdI):"memory"); }
    const unsigned wv=(lane>=26?ROWB+(lane-26)*16:lane*16)+nt*WPITCH;
    const unsigned wrow0=(ks*1664+32*(q>>1)+4*(q&1))*ROWB;
    const unsigned wl=WOFF+q*16*WPITCH;
    dmaW8(wl,wv,srdW,wrow0);
    dmaW8(STG+wl,wv,srdW,wrow0+64*ROWB);
    const unsigned pfw=(lane<52)? (ks*1664+4*j+lane/13)*ROWB+(xcd>>1)*1664+(lane%13)*128 : 0x7ffffff0u;
    const bool doA=(q==nl), doW=(q==((nl+1)&3));
    if(PFD && doW){ for(int f=1;f<=PFD;f++) pfload(pfw,srdW,f*128*ROWB); waitv<16+PFD>(); }
    else waitv<16>();
    asm volatile("":"+v"(iv[0]),"+v"(iv[1]),"+v"(iv[2]),"+v"(iv[3]),"+v"(ix));
    if(c8<7){
      #pragma unroll
      for(int p=0;p<4;p++){ ldsw<unsigned>(L_IDX+((2*c8)*128+q*32+r8*4+p)*4,iv[p][0]<<8); ldsw<unsigned>(L_IDX+((2*c8+1)*128+q*32+r8*4+p)*4,iv[p][1]<<8); }
    }
    unsigned asw[2];
    asw[0]=(c8^((r8>>1)&7))*16; asw[1]=(c8^(((8+r8)>>1)&7))*16;
    const unsigned t4=L_IDX+(q*8+r8)*16;
    u32x4 ro=ldsr<u32x4>(t4);
    #pragma unroll
    for(int p=0;p<4;p++) dma((4*q+p)*1024,ro[p]+asw[p&1],srdT,0);
    #pragma unroll
    for(int p=0;p<4;p++) dma(STG+(4*q+p)*1024,ro[p]+asw[p&1],srdT,128);
    const unsigned tp=L_IDX+(q*32+((lane>>1)&7)*4+(lane>>4))*4;
    const unsigned pfa=(lane&1)*128;
    load_ep();
    if(wg<8) bar();
    if(PFD && doA){ for(int f=1;f<=PFD;f++) pfload(ldsr<unsigned>(tp+f*512)+pfa,srdT,0); waitv<4+PFD>(); }
    else waitv<4>();
    STAMP(1);
    bar();
    if(tid==256) __hip_atomic_store((GAS unsigned*)(ws+W_STARTED)+wg,ep,RLX_AGENT);
    unsigned slot=2*STG;
    auto iter=[&](int f){
      ro=ldsr<u32x4>(t4+f*512);
      unsigned so=wrow0+f*128*ROWB;
      #pragma unroll
      for(int p=0;p<4;p++) dma(slot+(4*q+p)*1024,ro[p]+asw[p&1],srdT,0);
      dmaW8(slot+wl,wv,srdW,so);
      slot=(slot==2*STG)?0:slot+STG;
      if(PFD && (doA||doW) && f>1 && f-1+PFD<13) waitv<13>(); else waitv<12>();
      bar();
      #pragma unroll
      for(int p=0;p<4;p++) dma(slot+(4*q+p)*1024,ro[p]+asw[p&1],srdT,128);
      dmaW8(slot+wl,wv,srdW,so+64*ROWB);
      slot=(slot==2*STG)?0:slot+STG;
      if(PFD && (doA||doW) && f+PFD<13){
        if(doA) pfload(ldsr<unsigned>(tp+(f+PFD)*512)+pfa,srdT,0); else pfload(pfw,srdW,(f+PFD)*128*ROWB);
        waitv<13>();
      } else waitv<12>();
      bar();
    };
    for(int f=1;f<5;f++) iter(f);
    #pragma unroll
    for(int i=0;i<7;i++){ int r=4*i+(lane>>4); unsigned a=(unsigned)__builtin_amdgcn_ds_bpermute(r*4,(int)ix)*256u+(lane&15)*16;
      if(i<6) dma(L_X0+q*6656+i*1024,a,srdT,0);
      else dma_lo32(L_X0+q*6656+i*1024,a,srdT,0); }
    iter(5);
    { unsigned spins=0;
      while(true){ unsigned fl=__hip_atomic_load((GAS unsigned*)(ws+W_FFLAG)+lane,RLX_AGENT); if(__all(fl==ep)||++spins>SPIN_CAP) break; __builtin_amdgcn_s_sleep(1); } }
    __builtin_amdgcn_fence(__ATOMIC_ACQUIRE,"wavefront");
    u32x4 wf[12];
    #pragma unroll
    for(int i=0;i<12;i++) wf[i]=__builtin_amdgcn_raw_buffer_load_b128(rsW,W_FRAG+(q*12+i)*1024+lane*16,0,SC1);
    for(int f=6;f<13;f++) iter(f);
    STAMP(2);
    #pragma unroll
    for(int i=0;i<12;i++){ int qq=q*12+i; unsigned a=(qq<42? 2*STG+qq*1024 : L_IDX+(qq-42)*1024)+lane*16; ldsw<u32x4>(a,wf[i]); }
    waitv<0>();
    bar();
    barl();
    #pragma unroll
    for(int m=0;m<2;m++)
      #pragma unroll
      for(int n=0;n<7;n++) keep[m][n]=f32x4{0,0,0,0};
  } else {
    load_ep();
    if(tid==0) ldsw<unsigned>(L_QUEUE,0u);
    if(wg<16){
      const int i=4*(wg&7)+w, h=lane>>5, c=lane&31;
      const unsigned dm=L_X0+4096+w*4224;
      const unsigned* wo32=(const unsigned*)wout;
      if(wg<8){
        const rsrc_t rsC=mkrsrc(wc,3*17576*2);
        float p[4]={0,0,0,0};
        u32x2 v3[26];
        #pragma unroll
        for(int g=0;g<26;g++) v3[g]=__builtin_amdgcn_raw_buffer_load_b64(rsC,tid<169?(2*17576+g*676+4*tid)*2:0x7ffffff0,0,0);
        unsigned a16[16];
        #pragma unroll
        for(int k=0;k<16;k++){ int g=16*(k>>3)+8*h+(k&7); bool ok=g<26&&c<26&&i<26; a16[k]=__builtin_amdgcn_raw_buffer_load_b16(rsC,ok?(17576+g*676+i*26+c)*2:0x7ffffff0,0,0); }
        #pragma unroll
        for(int k=0;k<4;k++){ int e=tid+256*k, g=e>>5, b=e&31; if(g>=26||b>=27||(b==26&&false)) ldsw<float>(L_X0+e*4,0.f); }
        { unsigned o2=wout[26+(tid<26?tid:0)]; if(tid<26) ldsw<float>(L_X0+(tid*32+26)*4,bf2f(o2)); }
        #pragma unroll
        for(int g=0;g<26;g++){ unsigned ow=wo32[26+(g>>1)]; float o=(g&1)?bfhi(ow):bflo(ow);
          p[0]+=o*bflo(v3[g][0]); p[1]+=o*bfhi(v3[g][0]); p[2]+=o*bflo(v3[g][1]); p[3]+=o*bfhi(v3[g][1]); }
        if(tid<169){
          #pragma unroll
          for(int r=0;r<4;r++){ int e=4*tid+r; ldsw<float>(L_X0+((e/26)*32+e%26)*4,p[r]); } }
        barl();
        f32x16 D={};
        #pragma unroll
        for(int s=0;s<2;s++){
          u32x4 af,bh,bl;
          #pragma unroll
          for(int k=0;k<4;k++){
            af[k]=a16[8*s+2*k]|(a16[8*s+2*k+1]<<16);
            float p0=ldsr<float>(L_X0+((16*s+8*h+2*k)*32+c)*4), p1=ldsr<float>(L_X0+((16*s+8*h+2*k+1)*32+c)*4);
            bh[k]=cvtpk(p0,p1); bl[k]=cvtpk(p0-bflo(bh[k]),p1-bfhi(bh[k]));
          }
          D=MFMA32(__builtin_bit_cast(bf16x8,af),__builtin_bit_cast(bf16x8,bh),D);
          D=MFMA32(__builtin_bit_cast(bf16x8,af),__builtin_bit_cast(bf16x8,bl),D);
        }
        #pragma unroll
        for(int r=0;r<16;r++) ldsw<float>(dm+(((r&3)+8*(r>>2)+4*h)*33+c)*4,D[r]);
      } else {
        const unsigned* w132=(const unsigned*)wc+(i<26?i:0)*338;
        unsigned v1[6];
        #pragma unroll
        for(int k=0;k<6;k++){ int e=lane+64*k; v1[k]=w132[e<338?e:0]; }
        #pragma unroll
        for(int k=0;k<6;k++){ int e=lane+64*k; if(e<338){ int a=e/13,b=2*(e%13); ldsw<float>(dm+(a*33+b)*4,bflo(v1[k])); ldsw<float>(dm+(a*33+b+1)*4,bfhi(v1[k])); } }
      }
      { const int hh=lane&1, s=lane>>1; float v[8];
        #pragma unroll
        for(int k=0;k<8;k++){
          float x=0.f;
          if(s<23){ int t=8*s+k,u=t>>1,dl=u/13,a=u-13*dl,b=a+dl; b=b>=13?b-13:b; b+=13*(k&1);
            int pa=a+13*hh, pb=b+13*hh; pb=pb>=26?pb-26:pb;
            float x0=ldsr<float>(dm+(pa*33+pb)*4), x1=ldsr<float>(dm+(pb*33+pa)*4);
            x=x0+(pa!=pb?x1:0.f); if(t>=182||(hh&&(k&1)&&dl==0)) x=0.f;
          } else if(s<25){ int a=8*(s-23)+k; float x0=ldsr<float>(dm+(((a<13?a:0)+13*hh)*33+26)*4); x=a<13?x0:0.f; }
          v[k]=i<26?x:0.f;
        }
        u32x4 o; o[0]=cvtpk(v[0],v[1]); o[1]=cvtpk(v[2],v[3]); o[2]=cvtpk(v[4],v[5]); o[3]=cvtpk(v[6],v[7]);
        int fq= s<23? 2*s+(wg<8?1:0) : 46+(s-23);
        bool on= s<23 || (s<25&&wg<8);
        __builtin_amdgcn_raw_buffer_store_b128(o,rsW,on?W_FRAG+fq*1024+(hh*32+i)*16:0x7ffffff0,0,SC1);
      }
      waitv<0>();
      if(lane==0) __hip_atomic_store((GAS unsigned*)(ws+W_FFLAG)+wg*4+w,ep,RLX_AGENT);
    }
    f32x4 acc[4][7]={};
    unsigned aA0,aA1,aB0,aB1,aB6;
    { int row=wm*64+(lane&15); int s=(row>>1)&7;
      aA0=row*128+(((4*wn+(lane>>4))^s)*16); aA1=row*128+(((4*(wn^1)+(lane>>4))^s)*16);
      int trrow=4*(lane>>4)+((lane&15)>>2);
      unsigned b=WOFF+trrow*WPITCH+(lane&3)*8;
      aB0=b+32*wn*WPITCH+wn*224; aB1=b+32*(wn^1)*WPITCH+wn*224; aB6=b+32*wn*WPITCH+192; }
    bf16x8 A0[4],B0[7],A1[4],B1[6];
    barl();
    STAMP(1);
    #pragma unroll
    for(int m=0;m<4;m++) A0[m]=ldsr<bf16x8>(aA0+m*2048);
    #pragma unroll
    for(int n=0;n<6;n++) B0[n]=ldB(aB0+n*32);
    B0[6]=ldB(aB6);
    unsigned slot=0;
    for(int s=0;s<NSTG;s++){
      {
        unsigned a1=aA1+slot,b1a=aB1+slot;
        #pragma unroll
        for(int n=0;n<7;n++){
          #pragma unroll
          for(int m=0;m<4;m++){
            acc[m][n]=MFMA16(A0[m],B0[n],acc[m][n]);
            int i=n*4+m; SB0();
            if(i<3) B1[i]=ldB(b1a+i*32);
            else if(i<7) A1[i-3]=ldsr<bf16x8>(a1+(i-3)*2048);
            else if(i<10) B1[i-4]=ldB(b1a+(i-4)*32);
            SB0();
            if(i==19){ barl(); SB0(); }
          }
        }
      }
      slot=(slot==2*STG)?0:slot+STG;
      {
        unsigned a0=aA0+slot,b0=aB0+slot,b6=aB6+slot;
        bf16x8 nA[4],nB[7];
        #pragma unroll
        for(int n=0;n<6;n++){
          #pragma unroll
          for(int m=0;m<4;m++){
            acc[m][n]=MFMA16(A1[m],B1[n],acc[m][n]);
            int i=n*4+m; SB0();
            if(i<3) nB[i]=ldB(b0+i*32);
            else if(i<7) nA[i-3]=ldsr<bf16x8>(a0+(i-3)*2048);
            else if(i<10) nB[i-4]=ldB(b0+(i-4)*32);
            else if(i==10) nB[6]=ldB(b6);
            SB0();
          }
        }
        #pragma unroll
        for(int m=0;m<4;m++) A0[m]=nA[m];
        #pragma unroll
        for(int n=0;n<7;n++) B0[n]=nB[n];
      }
    }
    STAMP(2);
    #pragma unroll
    for(int n=0;n<7;n++){
      f32x4 s0=ks?acc[0][n]:acc[2][n], s1=ks?acc[1][n]:acc[3][n];
      keep[0][n]=ks?acc[2][n]:acc[0][n]; keep[1][n]=ks?acc[3][n]:acc[1][n];
      u32x4 o; o[0]=cvtpk(s0[0],s0[1]); o[1]=cvtpk(s0[2],s0[3]); o[2]=cvtpk(s1[0],s1[1]); o[3]=cvtpk(s1[2],s1[3]);
      __builtin_amdgcn_raw_buffer_store_b128(o,rsW,W_XBUF+((wg*4+w)*7+n)*1024+lane*16,0,SC1);
    }
    ldsw<f32x4>(L_B6+w*1024+lane*16, wn?keep[0][6]:keep[1][6]);
    waitv<0>();
    if(lane==0) __hip_atomic_store((GAS unsigned*)(ws+W_XFLAG+(wg*4+w)*64),ep,RLX_AGENT);
    STAMP(3);
  }
  barl();
  if(w<4){
    u16 bb[7],ww[7];
    #pragma unroll
    for(int n=0;n<7;n++){ int col=nt*208+(n<6?(wn*7+n):6)*16+(lane&15); bb[n]=b1[col]; ww[n]=w2[col]; }
    f32x4 o6=ldsr<f32x4>(L_B6+(w^1)*1024+lane*16);
    const int pw=(wg^1)*4+w;
    { unsigned spins=0;
      while(true){ unsigned fl=__hip_atomic_load((GAS unsigned*)(ws+W_XFLAG+(pw^(lane&1))*64),RLX_AGENT); if(__all(fl==ep)||++spins>SPIN_CAP) break; __builtin_amdgcn_s_sleep(1); } }
    __builtin_amdgcn_fence(__ATOMIC_ACQUIRE,"wavefront");
    STAMP(4);
    u32x4 pr[8];
    #pragma unroll
    for(int n=0;n<7;n++) pr[n]=__builtin_amdgcn_raw_buffer_load_b128(rsW,W_XBUF+(pw*7+n)*1024+lane*16,0,SC1);
    pr[7]=__builtin_amdgcn_raw_buffer_load_b128(rsW,W_XBUF+((pw^1)*7+6)*1024+lane*16,0,SC1);
    float rs[8]={0,0,0,0,0,0,0,0};
    #pragma unroll
    for(int n=0;n<6;n++){ float b=bf2f(bb[n]), wv=bf2f(ww[n]);
      #pragma unroll
      for(int m=0;m<2;m++){
        rs[4*m+0]+=gelu_w(keep[m][n][0]+bflo(pr[n][2*m])+b,wv);   rs[4*m+1]+=gelu_w(keep[m][n][1]+bfhi(pr[n][2*m])+b,wv);
        rs[4*m+2]+=gelu_w(keep[m][n][2]+bflo(pr[n][2*m+1])+b,wv); rs[4*m+3]+=gelu_w(keep[m][n][3]+bfhi(pr[n][2*m+1])+b,wv);
      } }
    { float b=bf2f(bb[6]), wv=bf2f(ww[6]);
      f32x4 k6=wn?keep[1][6]:keep[0][6];
      unsigned pa0=wn?pr[6][2]:pr[6][0], pa1=wn?pr[6][3]:pr[6][1], pb0=wn?pr[7][2]:pr[7][0], pb1=wn?pr[7][3]:pr[7][1];
      float g0=gelu_w(k6[0]+o6[0]+bflo(pa0)+bflo(pb0)+b,wv), g1=gelu_w(k6[1]+o6[1]+bfhi(pa0)+bfhi(pb0)+b,wv);
      float g2=gelu_w(k6[2]+o6[2]+bflo(pa1)+bflo(pb1)+b,wv), g3=gelu_w(k6[3]+o6[3]+bfhi(pa1)+bfhi(pb1)+b,wv);
      if(wn){ rs[4]+=g0; rs[5]+=g1; rs[6]+=g2; rs[7]+=g3; } else { rs[0]+=g0; rs[1]+=g1; rs[2]+=g2; rs[3]+=g3; } }
    #pragma unroll
    for(int k=0;k<8;k++) rs[k]=sum16(rs[k]);
    { const int c=lane&15; float v=rs[0];
      #pragma unroll
      for(int k=1;k<8;k++) v=(c==k)?rs[k]:v;
      int owner=mt*32+wm*16+(2*ks+(c>>2))*4+(lane>>4);
      if(c<8) __hip_atomic_store((GAS u64*)(ws+W_GRAN+owner*1024+(nt*2+wn)*32+(c&3)*8),((u64)ep<<32)|__float_as_uint(v),RLX_AGENT);
    }
    STAMP(5);
  }
  { const int h=lane>>5, c=lane&31;
    f32x16 vinit;
    #pragma unroll
    for(int r=0;r<16;r++){ int row=(r&3)+8*(r>>2)+4*h; vinit[r]=row<26?bf2f(wout[row<26?row:0]):0.f; }
    while(true){
      unsigned g=0; if(lane==0) g=__hip_atomic_fetch_add((LAS unsigned*)(size_t)L_QUEUE,1u,__ATOMIC_RELAXED,__HIP_MEMORY_SCOPE_WORKGROUP);
      g=__builtin_amdgcn_readfirstlane(g);
      if(g>=16) break;
      const unsigned xb=L_X0+(g>>2)*6656+((g&3)*32+c)*2;
      const unsigned x0a=xb+h*13*256, x0b=xb+(1-h)*13*256;
      float R[26];
      #pragma unroll
      for(int i=0;i<13;i++){ R[i]=bf2f(ldsr<u16>(x0a+i*256)); R[13+i]=bf2f(ldsr<u16>(x0b+i*256)); }
      f32x16 x={}, v=vinit;
      cin_steps<0>(R,lane*16,x,v);
      { u32x4 z; z[0]=cvtpk(R[0],R[1]); z[1]=cvtpk(R[2],R[3]); z[2]=cvtpk(R[4],R[5]); z[3]=cvtpk(R[6],R[7]);
        v=MFMA32(ldsr<bf16x8>(lane*16+wfrag_addr(46)),__builtin_bit_cast(bf16x8,z),v);
        z[0]=cvtpk(R[8],R[9]); z[1]=cvtpk(R[10],R[11]); z[2]=cvtpk(R[12],0.f); z[3]=0;
        v=MFMA32(ldsr<bf16x8>(lane*16+wfrag_addr(47)),__builtin_bit_cast(bf16x8,z),v); }
      float s=0.f;
      #pragma unroll
      for(int r=0;r<16;r++) s+=x[r]*v[r];
      s=sum64(s);
      if(lane==0) ldsw<float>(L_CINSUM+g*4,s);
    }
  }
  STAMP(6);
  barl();
  if(w==4){
    const GAS u64* gp=(const GAS u64*)(ws+W_GRAN+wg*1024)+lane*2;
    u64 g0,g1; unsigned spins=0;
    while(true){ g0=__hip_atomic_load(gp,RLX_AGENT); g1=__hip_atomic_load(gp+1,RLX_AGENT);
      if(__all((unsigned)(g0>>32)==ep && (unsigned)(g1>>32)==ep)||++spins>SPIN_CAP) break; __builtin_amdgcn_s_sleep(1); }
    float s0=__uint_as_float((unsigned)g0), s1=__uint_as_float((unsigned)g1);
    #pragma unroll
    for(int o=2;o<64;o<<=1){ s0+=__shfl_xor(s0,o); s1+=__shfl_xor(s1,o); }
    f32x4 c0=ldsr<f32x4>(L_CINSUM+(lane&1)*32), c1=ldsr<f32x4>(L_CINSUM+(lane&1)*32+16);
    float bo=bf2f(bout[0]);
    s0+=(c0[0]+c0[1])+(c0[2]+c0[3])+bo; s1+=(c1[0]+c1[1])+(c1[2]+c1[3])+bo;
    if(spins>SPIN_CAP){ s0=__uint_as_float(0x7fc00000u); s1=s0; }
    if(lane<2) ((unsigned*)out)[wg*2+lane]=cvtpk(s0,s1);
    STAMP(7);
  }
  if(wg==0 && w==5){
    unsigned spins=0; const GAS unsigned* sp=(const GAS unsigned*)(ws+W_STARTED)+lane;
    while(true){ bool ok=true;
      #pragma unroll
      for(int k=0;k<4;k++) ok&=__hip_atomic_load(sp+64*k,RLX_AGENT)==ep;
      if(__all(ok)||++spins>SPIN_CAP) break; __builtin_amdgcn_s_sleep(1); }
    if(lane==0) __hip_atomic_store((GAS unsigned*)(ws+W_EPOCH),ep,RLX_AGENT);
  }
}
extern "C" void kernel_launch(void* const* d_in,const int* in_sizes,int n_in,void* d_out,int out_size,void* d_ws,size_t ws_size,hipStream_t stream){
  (void)hipFuncSetAttribute((const void*)k_main,hipFuncAttributeMaxDynamicSharedMemorySize,L_TOTAL);
  k_main<<<256,512,L_TOTAL,stream>>>((const int*)d_in[0],(const u16*)d_in[1],(const u16*)d_in[2],(const u16*)d_in[3],(const u16*)d_in[4],(const u16*)d_in[5],(const u16*)d_in[6],(const u16*)d_in[7],(u16*)d_out,(char*)d_ws);
}
```

```cpp
#include <hip/hip_runtime.h>
#include <hip/hip_bf16.h>
typedef unsigned short u16;
#define THR 512
#define LDSB 163840
#define BIGV 0
#define TSPIN 400000
__device__ __forceinline__ float bf2f(u16 x){ return __uint_as_float(((unsigned)x)<<16); }
constexpr int F_=26,D_=128,DIN=3328;
__global__ __launch_bounds__(THR) void k(const int* idx,const u16* table,const u16* wc,const u16* wout,const u16* w1,const u16* b1,const u16* w2,const u16* bout,__hip_bfloat16* out){
  unsigned long long t0=__builtin_amdgcn_s_memrealtime();
  extern __shared__ float sm[];
  if(BIGV) asm volatile("v_mov_b32 v250, 0":::"v250");
  u16* xs=(u16*)sm;
  float* red=sm+6656;
  float* res=red+THR;
  const int t=threadIdx.x, wg=blockIdx.x;
  for(int e=t;e<4*DIN;e+=THR){ int s=e/DIN,r=e%DIN; xs[e]=table[(size_t)idx[(wg*4+s)*F_+(r>>7)]*D_+(r&127)]; }
  __syncthreads();
  float tot0=0,tot1=0,tot2=0,tot3=0;
  for(int n=t;n<DIN;n+=THR){
    float a0=0,a1=0,a2=0,a3=0;
    #pragma unroll 8
    for(int kk=0;kk<DIN;kk++){ float w=bf2f(w1[(size_t)kk*DIN+n]); a0+=bf2f(xs[kk])*w; a1+=bf2f(xs[DIN+kk])*w; a2+=bf2f(xs[2*DIN+kk])*w; a3+=bf2f(xs[3*DIN+kk])*w; }
    float bb=bf2f(b1[n]), ww=bf2f(w2[n]);
    auto g=[&](float x){ x+=bb; float u=0.7978845608f*(x+0.044715f*x*x*x); return x/(1.f+__expf(-2.f*u))*ww; };
    tot0+=g(a0); tot1+=g(a1); tot2+=g(a2); tot3+=g(a3);
  }
  float* xa=res+4; float* xb=xa+26*128;
  float cin[4]={0,0,0,0};
  #pragma unroll
  for(int s=0;s<4;s++){
    __syncthreads();
    for(int e=t;e<26*128;e+=THR) xa[e]=bf2f(xs[s*DIN+e]);
    __syncthreads();
    float* cur=xa; float* nxt=xb; float acc=0;
    for(int l=0;l<3;l++){
      for(int e=t;e<26*128;e+=THR){ int h=e>>7,d=e&127; float v=0;
        for(int i=0;i<26;i++){ float tt=0; for(int j=0;j<26;j++) tt+=bf2f(wc[((l*26+h)*26+i)*26+j])*bf2f(xs[s*DIN+j*128+d]); v+=tt*cur[i*128+d]; }
        nxt[e]=v; acc+=v*bf2f(wout[l*26+h]); }
      __syncthreads(); float* tmp=cur; cur=nxt; nxt=tmp;
    }
    cin[s]=acc;
  }
  float tt[4]={tot0+cin[0],tot1+cin[1],tot2+cin[2],tot3+cin[3]};
  #pragma unroll
  for(int s=0;s<4;s++){ __syncthreads(); red[t]=tt[s]; __syncthreads(); if(t==0){ float a=0; for(int i=0;i<THR;i++) a+=red[i]; out[wg*4+s]=__float2bfloat16(a+bf2f(bout[0])); } }
  while(__builtin_amdgcn_s_memrealtime()-t0 < TSPIN) __builtin_amdgcn_s_sleep(2);
}
extern "C" void kernel_launch(void* const* d_in,const int* in_sizes,int n_in,void* d_out,int out_size,void* d_ws,size_t ws_size,hipStream_t stream){
  (void)hipFuncSetAttribute((const void*)k,hipFuncAttributeMaxDynamicSharedMemorySize,LDSB);
  k<<<256,THR,LDSB,stream>>>((const int*)d_in[0],(const u16*)d_in[1],(const u16*)d_in[2],(const u16*)d_in[3],(const u16*)d_in[4],(const u16*)d_in[5],(const u16*)d_in[6],(const u16*)d_in[7],(__hip_bfloat16*)d_out);
}
```

```cpp
#include <hip/hip_runtime.h>
#include <hip/hip_bf16.h>
typedef unsigned short u16;
typedef unsigned long long u64;
typedef int i32x4 __attribute__((ext_vector_type(4)));
typedef unsigned u32x4 __attribute__((ext_vector_type(4)));
typedef unsigned u32x2 __attribute__((ext_vector_type(2)));
typedef short s16x4 __attribute__((ext_vector_type(4)));
typedef __bf16 bf16x8 __attribute__((ext_vector_type(8)));
typedef __bf16 bf16x2 __attribute__((ext_vector_type(2)));
typedef float f32x2 __attribute__((ext_vector_type(2)));
typedef float f32x4 __attribute__((ext_vector_type(4)));
typedef float f32x16 __attribute__((ext_vector_type(16)));
#define DI __device__ __forceinline__
#define LAS __attribute__((address_space(3)))
#define GAS __attribute__((address_space(1)))
#define STAMPS 0
#define CODEPF 27648
#define TLBW 1
#define SPINLOG 18
#define NOCLR 0
#define NOCIN 0
#define THR 63
#define STR_(x) #x
#define STR(x) STR_(x)
#define THRW "s_waitcnt vmcnt(" STR(THR) ")\n\t"
#define PRIO 0
#define LPRIO 1
constexpr int NB=1024,NF=26,ND=128,DIN=3328,ROWB=DIN*2;
constexpr int ASLOT=16384, WSLOT=26624, WPITCH=416, L_W=3*ASLOT;
constexpr int L_A2=2*ASLOT, L_W2=L_W+2*WSLOT;
constexpr int L_CW=L_W+3*WSLOT, L_IDX=L_CW+26624, L_MISC=L_IDX+7168, L_TOTAL=163840;
constexpr int L_CNT=L_MISC, L_O1=L_MISC+128, L_PFD=L_MISC+768;
constexpr int L_B6=L_A2;
constexpr int L_F3=L_W2, L_FA=L_W2+35840, L_FP=L_FA+8192, L_FD=L_W2;
static_assert(L_FP+4096<=L_IDX && L_MISC+1024==L_TOTAL,"lds map");
constexpr unsigned W_EPOCH=0, W_STARTED=256, W_FFLAG=2048, W_XFLAG=4096, W_FRAG=4096+65536, W_GRAN=W_FRAG+49152, W_XBUF=W_GRAN+262144, W_END=W_XBUF+256*4*7168;
constexpr unsigned SPIN_CAP=1u<<SPINLOG, OOB=0x7ffffff0u;

DI float bf2f(unsigned x){ return __uint_as_float(x<<16); }
DI float bflo(unsigned x){ return __uint_as_float(x<<16); }
DI float bfhi(unsigned x){ return __uint_as_float(x&0xffff0000u); }
DI unsigned cvtpk(float lo,float hi){ f32x2 v={lo,hi}; bf16x2 b=__builtin_convertvector(v,bf16x2); return __builtin_bit_cast(unsigned,b); }
DI void dma(unsigned m0v, unsigned voff, i32x4 srd, unsigned soff){
  asm volatile("s_mov_b32 m0, %0\n\t" THRW "buffer_load_dwordx4 %1, %2, %3 offen lds" :: "s"(m0v), "v"(voff), "s"(srd), "s"(soff) : "memory");
}
DI void dma_sc1(unsigned m0v, unsigned voff, i32x4 srd, unsigned soff){
  asm volatile("s_mov_b32 m0, %0\n\ts_nop 0\n\tbuffer_load_dwordx4 %1, %2, %3 offen sc1 lds" :: "s"(m0v), "v"(voff), "s"(srd), "s"(soff) : "memory");
}
DI void dma_lo16(unsigned m0v, unsigned voff, i32x4 srd, unsigned soff){
  asm volatile("s_mov_b32 m0, %0\n\ts_mov_b64 exec, 0xffff\n\tbuffer_load_dwordx4 %1, %2, %3 offen lds\n\ts_mov_b64 exec, -1" :: "s"(m0v), "v"(voff), "s"(srd), "s"(soff) : "memory");
}
constexpr u64 MASK52=0x000FFFFFFFFFFFFFull;
template<int I> DI void dmaW(unsigned m0v, unsigned voff, i32x4 srd, unsigned so){
  unsigned t;
  asm volatile("s_add_u32 m0, %1, %6\n\ts_add_u32 %0, %4, %7\n\ts_mov_b64 exec, %5\n\t" THRW "buffer_load_dwordx4 %2, %3, %0 offen lds\n\ts_mov_b64 exec, -1"
    : "=&s"(t) : "s"(m0v), "v"(voff), "s"(srd), "s"(so), "s"(MASK52), "n"(832*I), "n"((8*(I>>1)+2*(I&1))*ROWB) : "memory","scc");
}
DI void dmaW8(unsigned m0v, unsigned voff, i32x4 srd, unsigned so){
  dmaW<0>(m0v,voff,srd,so); dmaW<1>(m0v,voff,srd,so); dmaW<2>(m0v,voff,srd,so); dmaW<3>(m0v,voff,srd,so);
  dmaW<4>(m0v,voff,srd,so); dmaW<5>(m0v,voff,srd,so); dmaW<6>(m0v,voff,srd,so); dmaW<7>(m0v,voff,srd,so);
}
DI void pfload(unsigned voff, i32x4 srd, unsigned soff){
  asm volatile("s_mov_b32 m0, %3\n\ts_nop 0\n\tbuffer_load_dword %0, %1, %2 offen lds" :: "v"(voff), "s"(srd), "s"(soff), "n"(L_PFD) : "memory"); }
DI void ld4_sc1(u32x4& d,unsigned voff,i32x4 srd,unsigned soff){ asm volatile("buffer_load_dwordx4 %0, %1, %2, %3 offen sc1":"=v"(d):"v"(voff),"s"(srd),"s"(soff):"memory"); }
DI void st4_sc1(u32x4 d,unsigned voff,i32x4 srd,unsigned soff){ asm volatile("buffer_store_dwordx4 %0, %1, %2, %3 offen sc1\n\ts_nop 1"::"v"(d),"v"(voff),"s"(srd),"s"(soff):"memory"); }
DI void ld1_sc1(unsigned& d,unsigned voff,i32x4 srd,unsigned soff){ asm volatile("buffer_load_dword %0, %1, %2, %3 offen sc1":"=v"(d):"v"(voff),"s"(srd),"s"(soff):"memory"); }
DI void ld4(u32x4& d,unsigned voff,i32x4 srd,unsigned soff){ asm volatile("buffer_load_dwordx4 %0, %1, %2, %3 offen":"=v"(d):"v"(voff),"s"(srd),"s"(soff):"memory"); }
DI void ldh(unsigned& d,unsigned voff,i32x4 srd,unsigned soff){ asm volatile("buffer_load_ushort %0, %1, %2, %3 offen":"=v"(d):"v"(voff),"s"(srd),"s"(soff):"memory"); }
DI void ld1(unsigned& d,unsigned voff,i32x4 srd,unsigned soff){ asm volatile("buffer_load_dword %0, %1, %2, %3 offen":"=v"(d):"v"(voff),"s"(srd),"s"(soff):"memory"); }
template<class T> DI void pin(T& x){ asm volatile("":"+v"(x)); }
template<int N> DI void waitv(){ asm volatile("s_waitcnt vmcnt(%0)"::"n"(N):"memory"); }
DI void bar(){ asm volatile("s_barrier":::"memory"); }
DI void barl(){ asm volatile("s_waitcnt lgkmcnt(0)\n\ts_barrier":::"memory"); }
DI i32x4 mksrd(const void* p, unsigned bytes){
  u64 a=(u64)p; i32x4 s;
  s[0]=__builtin_amdgcn_readfirstlane((unsigned)a); s[1]=__builtin_amdgcn_readfirstlane((unsigned)(a>>32)&0xffff); s[2]=bytes; s[3]=0x20000; return s;
}
typedef __amdgpu_buffer_rsrc_t rsrc_t;
DI rsrc_t mkrsrc(const void* p, unsigned bytes){ return __builtin_amdgcn_make_buffer_rsrc((void*)p,0,bytes,0x20000); }
#define SC1 16
#define RLX_AGENT __ATOMIC_RELAXED, __HIP_MEMORY_SCOPE_AGENT
#define SB0() __builtin_amdgcn_sched_barrier(0)
#define STAMP(i)
#define TS(i)
#define TSOUT(i)
#define DBG(code,cond)
template<class T> DI T ldsr(unsigned a){ return *(const LAS T*)(size_t)a; }
template<class T> DI void ldsw(unsigned a,T v){ *(LAS T*)(size_t)a=v; }
DI s16x4 ldT(unsigned a){ return __builtin_amdgcn_ds_read_tr16_b64_v4i16((LAS s16x4*)(size_t)a); }
DI bf16x8 cat(s16x4 lo,s16x4 hi){ return __builtin_bit_cast(bf16x8,__builtin_shufflevector(lo,hi,0,1,2,3,4,5,6,7)); }
#define MFMA16(a,b,c) __builtin_amdgcn_mfma_f32_16x16x32_bf16(a,b,c,0,0,0)
#define MFMA32(a,b,c) __builtin_amdgcn_mfma_f32_32x32x16_bf16(a,b,c,0,0,0)
#define BC8(x) __builtin_bit_cast(bf16x8,x)
template<int CTRL> DI float dppadd(float v){ return v+__int_as_float(__builtin_amdgcn_update_dpp(0,__float_as_int(v),CTRL,0xf,0xf,true)); }
DI float sum16(float v){ v=dppadd<0xB1>(v); v=dppadd<0x4E>(v); v=dppadd<0x141>(v); v=dppadd<0x140>(v); return v; }
DI float rl(float v,int l){ return __int_as_float(__builtin_amdgcn_readlane(__float_as_int(v),l)); }
DI unsigned lds_inc(unsigned addr){
  unsigned old,r;
  asm volatile("s_mov_b64 exec, 1\n\tds_add_rtn_u32 %0, %2, %3\n\ts_mov_b64 exec, -1\n\ts_waitcnt lgkmcnt(0)\n\tv_readfirstlane_b32 %1, %0":"=&v"(old),"=s"(r):"v"(addr),"v"(1u):"memory");
  return r;
}
DI unsigned lds_peek(unsigned addr){ unsigned v,r;
  asm volatile("ds_read_b32 %0, %2\n\ts_waitcnt lgkmcnt(0)\n\tv_readfirstlane_b32 %1, %0":"=&v"(v),"=s"(r):"v"(addr):"memory"); return r; }
DI void lds_arrive(unsigned cnt){ asm volatile("s_waitcnt lgkmcnt(0)\n\ts_mov_b64 exec, 1\n\tds_add_u32 %0, %1\n\ts_mov_b64 exec, -1"::"v"(cnt),"v"(1u):"memory"); }
DI bool lds_wait(unsigned cnt,unsigned n){ unsigned spins=0; while(lds_peek(cnt)<n && ++spins<=SPIN_CAP) __builtin_amdgcn_s_sleep(0); return spins>SPIN_CAP; }
DI bool lds_sync(unsigned cnt,unsigned n){
  asm volatile("s_waitcnt lgkmcnt(0)":::"memory");
  unsigned spins=0, v=lds_inc(cnt)+1u;
  while(v<n && ++spins<=SPIN_CAP){ __builtin_amdgcn_s_sleep(0); v=lds_peek(cnt); }
  return spins>SPIN_CAP;
}
DI void mfma16_acc(f32x4& c,bf16x8 a,bf16x8 b){ asm volatile("v_mfma_f32_16x16x32_bf16 %0, %1, %2, %0":"+v"(c):"v"(a),"v"(b)); }

template<int S,bool LAST> DI void gemm_stage(f32x4 (&acc)[4][7],bf16x8 (&A0)[4],s16x4 (&B0)[14],unsigned aA0,unsigned aA1,unsigned aB0,unsigned aB1,unsigned aB6){
  constexpr int SN=(S+1)%3, HI=16*WPITCH;
  bf16x8 A1[4]; s16x4 B1[12];
  #pragma unroll
  for(int i=0;i<28;i++){ const int n=i>>2,m=i&3;
    mfma16_acc(acc[m][n],A0[m],cat(B0[2*n],B0[2*n+1])); SB0();
    if(i<2) B1[i]=ldT(aB1+S*WSLOT+(i&1)*HI);
    else if(i<6) A1[i-2]=ldsr<bf16x8>(aA1+S*ASLOT+(i-2)*2048);
    else if(i<16){ const int k=i-4; B1[k]=ldT(aB1+S*WSLOT+(k>>1)*32+(k&1)*HI); }
    SB0();
    if(i==22 && !LAST){ barl(); SB0(); }
  }
  #pragma unroll
  for(int i=0;i<24;i++){ const int n=i>>2,m=i&3;
    mfma16_acc(acc[m][n],A1[m],cat(B1[2*n],B1[2*n+1])); SB0();
    if(!LAST){
      if(i<2) B0[i]=ldT(aB0+SN*WSLOT+(i&1)*HI);
      else if(i<6) A0[i-2]=ldsr<bf16x8>(aA0+SN*ASLOT+(i-2)*2048);
      else if(i<16){ const int k=i-4; B0[k]=ldT(aB0+SN*WSLOT+(k>>1)*32+(k&1)*HI); }
      else if(i<18) B0[i-4]=ldT(aB6+SN*WSLOT+(i&1)*HI);
    }
    SB0();
  }
}

template<int T> struct Slot { static constexpr int u=T>>1, a=T<182?u%13:0, b=T<182?(u%13+u/13)%13:0; };
DI void mfma32_acc(f32x16& c,u32x4 a,u32x4 b){ asm volatile("v_mfma_f32_32x32x16_bf16 %0, %1, %2, %0":"+v"(c):"v"(a),"v"(b)); }
DI void zq(unsigned& o0,unsigned& o1,float a0,f32x2 b0,float a1,f32x2 b1){
  float t0,t1,t2,t3;
  asm volatile("v_mul_f32_e32 %2, %6, %7\n\tv_mul_f32_e32 %3, %6, %8\n\tv_mul_f32_e32 %4, %9, %10\n\tv_mul_f32_e32 %5, %9, %11\n\tv_cvt_pk_bf16_f32 %0, %2, %3\n\tv_cvt_pk_bf16_f32 %1, %4, %5"
    :"=&v"(o0),"=&v"(o1),"=&v"(t0),"=&v"(t1),"=&v"(t2),"=&v"(t3):"v"(a0),"v"(b0[0]),"v"(b0[1]),"v"(a1),"v"(b1[0]),"v"(b1[1]));
}
DI void zl(unsigned& o0,unsigned& o1,float a,float b,float c,float d){ asm volatile("v_cvt_pk_bf16_f32 %0, %2, %3\n\tv_cvt_pk_bf16_f32 %1, %4, %5":"=&v"(o0),"=&v"(o1):"v"(a),"v"(b),"v"(c),"v"(d)); }
template<int S,int H> DI void zhalf(const f32x2 (&R)[13],u32x4& z){
  unsigned o0,o1;
  if constexpr(S<23){ using P=Slot<8*S+4*H>; using Q=Slot<8*S+4*H+2>; zq(o0,o1,R[P::a][0],R[P::b],R[Q::a][0],R[Q::b]); }
  else if constexpr(S==23) zl(o0,o1,R[4*H][0],R[4*H+1][0],R[4*H+2][0],R[4*H+3][0]);
  else if constexpr(S==24){ if constexpr(H==0) zl(o0,o1,R[8][0],R[9][0],R[10][0],R[11][0]); else zl(o0,o1,R[12][0],R[12][0],R[12][0],R[12][0]); }
  if constexpr(S<25){ z[2*H]=o0; z[2*H+1]=o1; }
}
struct Cin { f32x2 R[13]; f32x16 x,v; u32x4 z[2], wb[2], w22; unsigned lw; };
template<int S,int H,bool ON=true> DI void cin_half(Cin& c,const u32x4 (&wr)[22]){
  if constexpr(!ON){} else if constexpr(H==0){
    if constexpr(S<22) mfma32_acc(c.x,wr[S],c.z[S&1]); else if constexpr(S==22) mfma32_acc(c.x,c.w22,c.z[S&1]);
    SB0();
    if constexpr(S==20) c.w22=ldsr<u32x4>(c.lw+23*1024);
    zhalf<S+1,1>(c.R,c.z[(S+1)&1]);
    SB0();
  } else {
    mfma32_acc(c.v,c.wb[S&1],c.z[S&1]);
    SB0();
    if constexpr(S+2<23) c.wb[S&1]=ldsr<u32x4>(c.lw+(S+2)*1024); else if constexpr(S+2<25) c.wb[S&1]=ldsr<u32x4>(c.lw+(S+3)*1024);
    zhalf<S+2,0>(c.R,c.z[S&1]);
    SB0();
  }
}
DI void cin_begin(Cin& c,const unsigned (&raw)[26],bool hi,int h){
  c.wb[0]=ldsr<u32x4>(c.lw); c.wb[1]=ldsr<u32x4>(c.lw+1024);
  #pragma unroll
  for(int k=0;k<4;k++){ f32x4 o=ldsr<f32x4>(L_O1+k*32+h*16); c.v[4*k]=o[0]; c.v[4*k+1]=o[1]; c.v[4*k+2]=o[2]; c.v[4*k+3]=o[3]; }
  const unsigned sh=hi?0u:16u;
  #pragma unroll
  for(int i=0;i<13;i++){ c.R[i][0]=__uint_as_float((raw[i]<<sh)&0xffff0000u); c.R[i][1]=__uint_as_float((raw[13+i]<<sh)&0xffff0000u); }
  c.x=f32x16{};
  pin(c.x); pin(c.v);
  zhalf<0,0>(c.R,c.z[0]); zhalf<0,1>(c.R,c.z[0]); zhalf<1,0>(c.R,c.z[1]);
  SB0();
}
DI float cin_end(Cin& c){ f32x2 s={0.f,0.f};
  asm volatile("s_nop 7\n\ts_nop 3":"+v"(c.x),"+v"(c.v));
  #pragma unroll
  for(int r=0;r<8;r++){ f32x2 a={c.x[2*r],c.x[2*r+1]}, b={c.v[2*r],c.v[2*r+1]}; s+=a*b; }
  return s[0]+s[1]; }
template<int S> DI void cin_run(Cin& c,const u32x4 (&wr)[22]){ if constexpr(S<25){ cin_half<S,0>(c,wr); cin_half<S,1>(c,wr); cin_run<S+1>(c,wr); } }

struct GTab { u16 v[64*16]; };
constexpr GTab make_gtab(){ GTab t{};
  for(int c=0;c<64;c++) for(int k=0;k<8;k++){ int hh=c&1,s=c>>1,o0=1056,o1=1056;
    if(s<23){ int tt=8*s+k,u=tt>>1,dl=u/13,a=u%13,b=(a+dl)%13+13*(k&1),pa=a+13*hh,pb=(b+13*hh)%26;
      if(tt<182 && !(hh&&(k&1)&&dl==0)){ o0=pa*33+pb; if(pa!=pb) o1=pb*33+pa; } }
    else if(s<25){ int a=8*(s-23)+k; if(a<13) o0=(a+13*hh)*33+26; }
    t.v[c*16+2*k]=(u16)(o0*4); t.v[c*16+2*k+1]=(u16)(o1*4); }
  return t; }
__device__ const GTab g_gtab=make_gtab();
DI float gelu_w(float x,float w2){
  float t=x*(-2.302208198f-0.1029432397f*x*x);
  float e=__builtin_amdgcn_exp2f(t);
  return x*__builtin_amdgcn_rcpf(1.f+e)*w2;
}

struct Ldr { i32x4 srdT,srdW; unsigned wv,wl,al,t4,asw0,asw1,so,sa,sw,st; };
DI void ldr_next(Ldr& L){ L.sw=(L.sa==2*ASLOT)?0:L.sw+WSLOT; L.sa=(L.sa==2*ASLOT)?0:L.sa+ASLOT; L.so+=64*ROWB; L.st+=1; }
template<int P> DI void dmaA(Ldr& L,const u32x4& ro){ dma(L.sa+L.al+P*1024,ro[P]+((P&1)?L.asw1:L.asw0),L.srdT,(L.st&1)*128); }
DI void ldr_issue(Ldr& L){
  u32x4 ro=ldsr<u32x4>(L.t4+(L.st>>1)*512);
  dmaW8(L.sw+L.wl,L.wv,L.srdW,L.so);
  dmaA<0>(L,ro); dmaA<1>(L,ro); dmaA<2>(L,ro); dmaA<3>(L,ro);
  ldr_next(L);
}
template<int K> DI void ldr_cin(Ldr& L,Cin& c,const u32x4 (&wr)[22]){
  constexpr int S=K<4? 4*K : 16+3*(K-4);
  u32x4 ro=ldsr<u32x4>(L.t4+(L.st>>1)*512);
  const unsigned m=L.sw+L.wl;
  SB0();
  dmaW<0>(m,L.wv,L.srdW,L.so); SB0();
  cin_half<S,0,!NOCIN>(c,wr);
  dmaW<1>(m,L.wv,L.srdW,L.so); dmaW<2>(m,L.wv,L.srdW,L.so); SB0();
  cin_half<S,1,!NOCIN>(c,wr);
  dmaW<3>(m,L.wv,L.srdW,L.so); SB0();
  cin_half<S+1,0,!NOCIN>(c,wr);
  dmaW<4>(m,L.wv,L.srdW,L.so); dmaW<5>(m,L.wv,L.srdW,L.so); SB0();
  cin_half<S+1,1,!NOCIN>(c,wr);
  dmaW<6>(m,L.wv,L.srdW,L.so); SB0();
  cin_half<S+2,0,!NOCIN>(c,wr);
  dmaW<7>(m,L.wv,L.srdW,L.so); dmaA<0>(L,ro); SB0();
  cin_half<S+2,1,!NOCIN>(c,wr);
  dmaA<1>(L,ro); SB0();
  if constexpr(K<4) cin_half<S+3,0,!NOCIN>(c,wr);
  dmaA<2>(L,ro); dmaA<3>(L,ro); SB0();
  if constexpr(K<4) cin_half<S+3,1,!NOCIN>(c,wr);
  ldr_next(L);
  waitv<12>();
  bar();
  SB0();
}

__global__ __launch_bounds__(512,2) void k_main(const int* __restrict__ idx,const u16* __restrict__ table,const u16* __restrict__ wc,const u16* __restrict__ wout,
    const u16* __restrict__ w1,const u16* __restrict__ b1,const u16* __restrict__ w2,const u16* __restrict__ bout,u16* __restrict__ out,char* ws){
  extern __shared__ __attribute__((aligned(1024))) char lds[];
  const int tid=threadIdx.x, lane=tid&63, w=__builtin_amdgcn_readfirstlane(tid>>6);
  const int wg=blockIdx.x, xcd=wg&7, j=wg>>3, ks=xcd&1, mt=j&7, nl=j>>3, nt=(xcd>>1)*4+nl;
  if(wg<16){
    u64 pc; asm volatile("s_getpc_b64 %0":"=s"(pc));
    pfload((unsigned)(tid*128),mksrd((const void*)(pc&~255ull),CODEPF),0);
  }
  STAMP(0);
  asm volatile(""::"s"(idx),"s"(table),"s"(wc),"s"(wout),"s"(w1),"s"(b1),"s"(w2),"s"(bout),"s"(out),"s"(ws));
  unsigned ep;
  asm volatile("s_load_dword %0, %1, 0x0":"=s"(ep):"s"(ws):"memory");
  auto wait_ep=[&](){ asm volatile("s_waitcnt lgkmcnt(0)\n\ts_add_u32 %0, %0, 1":"+s"(ep)::"memory","scc"); };
  const i32x4 srdS=mksrd(ws,W_END);
  if(__builtin_expect(w>=4,1)){
    const int q=w-4;
    Ldr L; L.srdT=mksrd(table,100000u*256u); L.srdW=mksrd(w1,(unsigned)DIN*ROWB);
    const i32x4 srdI=mksrd(idx,NB*NF*4);
    const int r8=lane>>3, c8=lane&7, h=lane>>5;
    u32x2 iv[4]; unsigned i0[4], ix;
    const unsigned irow=((mt*128+((32*q)^(32*ks))+r8)*NF+ks*13)*4;
    #pragma unroll
    for(int p=0;p<4;p++) asm volatile("buffer_load_dword %0, %1, %2, 0 offen offset:%3":"=v"(i0[p]):"v"(irow),"s"(srdI),"n"(p*8*NF*4):"memory");
    #pragma unroll
    for(int p=0;p<4;p++) asm volatile("buffer_load_dwordx2 %0, %1, %2, 0 offen offset:%3":"=v"(iv[p]):"v"(irow+8*c8),"s"(srdI),"n"(p*8*NF*4):"memory");
    ld1(ix,(lane<26?lane:0)*4,srdI,(wg*4+q)*NF*4);
    if(q==0) pfload(lane<25? (unsigned)lane<<20 : OOB, L.srdT, 0);
    if(wg<16) bar();
    L.wv=(lane>=26?ROWB+(lane-26)*16:lane*16)+nt*WPITCH;
    L.so=(ks*1664+32*(q>>1)+4*(q&1))*ROWB;
    L.wl=L_W+q*16*WPITCH; L.al=q*4096;
    dmaW8(L.wl,L.wv,L.srdW,L.so);
    L.asw0=(c8^((r8>>1)&7))*16; L.asw1=L.asw0^64;
    waitv<8>();
    #pragma unroll
    for(int p=0;p<4;p++){ pin(i0[p]); pin(iv[p]); }
    pin(ix);
    #pragma unroll
    for(int p=0;p<4;p++){ i0[p]=(i0[p]<<8)+((p&1)?L.asw1:L.asw0); dma(L.al+p*1024,i0[p],L.srdT,0); }
    dmaW8(WSLOT+L.wl,L.wv,L.srdW,L.so+64*ROWB);
    #pragma unroll
    for(int p=0;p<4;p++) dma(ASLOT+L.al+p*1024,i0[p],L.srdT,128);
    if(c8<7){
      #pragma unroll
      for(int p=0;p<4;p++){ ldsw<unsigned>(L_IDX+((2*c8)*128+q*32+r8*4+p)*4,iv[p][0]<<8); ldsw<unsigned>(L_IDX+((2*c8+1)*128+q*32+r8*4+p)*4,iv[p][1]<<8); }
    }
    L.t4=L_IDX+(q*8+r8)*16; L.sa=2*ASLOT; L.sw=2*WSLOT; L.so+=128*ROWB; L.st=2;
    wait_ep();
    waitv<12>();
    STAMP(1);
    bar();
    __builtin_amdgcn_s_setprio(LPRIO);
    if(tid==256) __hip_atomic_store((GAS unsigned*)(ws+W_STARTED)+wg,ep,RLX_AGENT);
    unsigned raw[26];
    auto x0_dma=[&](unsigned base,unsigned half){
      pin(ix);
      #pragma unroll
      for(int i=0;i<4;i++){ unsigned a=(unsigned)__builtin_amdgcn_ds_bpermute((8*i+r8)*4,(int)ix)*256u+c8*16;
        if(i<3) dma(base+i*1024,a,L.srdT,half*128); else dma_lo16(base+i*1024,a,L.srdT,half*128); } };
    auto x0_read=[&](unsigned base){
      unsigned a0=base+h*13*128+(lane&31)*4, a1=base+(1-h)*13*128+(lane&31)*4;
      #pragma unroll
      for(int i=0;i<13;i++){ raw[i]=ldsr<unsigned>(a0+i*128); raw[13+i]=ldsr<unsigned>(a1+i*128); }
      #pragma unroll
      for(int i=0;i<26;i++) pin(raw[i]); };
    x0_dma(L_CW+q*3328,0);
    #pragma nounroll
    for(int s=0;s<6;s++){
      if(s<4){
        u32x4 ones={~0u,~0u,~0u,~0u};
        st4_sc1(ones,lane*16,srdS,W_XBUF+(wg*4+q)*7168+s*2048);
        if(s<3) st4_sc1(ones,lane*16+1024,srdS,W_XBUF+(wg*4+q)*7168+s*2048);
      }
      if(s==2 && wg<16 && tid==256) __hip_atomic_store((GAS unsigned*)(ws+W_FFLAG)+wg,ep,RLX_AGENT);
      if(s==4 && lane==0) __hip_atomic_store((GAS unsigned*)(ws+W_XFLAG+(wg*4+q)*64),ep,RLX_AGENT);
      ldr_issue(L); waitv<12>(); bar(); }
    STAMP(9);
    { unsigned fl=ep; if(q==0) ld1_sc1(fl,(lane&15)*4,srdS,W_FFLAG);
      ldr_issue(L);
      x0_read(L_CW+q*3328);
      waitv<12>(); pin(fl);
      unsigned spins=0; while(!__all(fl==ep) && ++spins<=SPIN_CAP){ __builtin_amdgcn_s_sleep(16); ld1_sc1(fl,(lane&15)*4,srdS,W_FFLAG); waitv<0>(); pin(fl); } DBG(3,spins>SPIN_CAP); }
    barl();
    STAMP(10);
    #pragma unroll
    for(int i=0;i<6;i++) dma_sc1(L_CW+(4*i+q)*1024,lane*16,srdS,W_FRAG+(4*i+q)*1024);
    ldr_issue(L); waitv<12>(); bar();
    Cin c; c.lw=L_CW+lane*16; pin(c.lw);
    u32x4 wr[22];
    ldr_issue(L);
    #pragma unroll
    for(int i=0;i<22;i++) wr[i]=ldsr<u32x4>(c.lw+i*1024);
    #pragma unroll
    for(int i=0;i<22;i++) pin(wr[i]);
    waitv<12>(); barl();
    STAMP(11);
    #pragma unroll
    for(int i=0;i<6;i++) dma_sc1(L_CW+(4*i+q)*1024,lane*16,srdS,W_FRAG+(22+4*i+q)*1024);
    if(q<2) dma_sc1(L_CW+(24+q)*1024,lane*16,srdS,W_FRAG+(46+q)*1024);
    ldr_issue(L); waitv<12>(); bar();
    STAMP(5);
    float cs=0.f;
    #pragma nounroll
    for(int g=0;g<2;g++){
      cin_begin(c,raw,g,h);
      ldr_cin<0>(L,c,wr); ldr_cin<1>(L,c,wr); ldr_cin<2>(L,c,wr); ldr_cin<3>(L,c,wr);
      ldr_cin<4>(L,c,wr); ldr_cin<5>(L,c,wr); ldr_cin<6>(L,c,wr);
      cs+=cin_end(c);
    }
    STAMP(2);
    __builtin_amdgcn_s_setprio(0);
    { unsigned pf; const unsigned po=W_XFLAG+((wg^1)*4+q)*64; ld1_sc1(pf,0,srdS,po);
      x0_dma(L_W2+q*3328,1);
      waitv<4>(); pin(pf);
      unsigned spins=0; while(__builtin_amdgcn_readfirstlane(pf)!=ep && ++spins<=SPIN_CAP){ __builtin_amdgcn_s_sleep(4); ld1_sc1(pf,0,srdS,po); waitv<0>(); pin(pf); } DBG(4,spins>SPIN_CAP); }
    bar();
    waitv<0>();
    x0_read(L_W2+q*3328);
    STAMP(8);
    #pragma nounroll
    for(int g=0;g<2;g++){
      cin_begin(c,raw,g,h);
      cin_run<0>(c,wr);
      cs+=cin_end(c);
    }
    STAMP(6);
    cs=sum16(cs); cs=(rl(cs,0)+rl(cs,16))+(rl(cs,32)+rl(cs,48));
    { u64 g0; unsigned spins=0; const GAS u64* gp=(const GAS u64*)(ws+W_GRAN+wg*1024+(lane&31)*32+q*8);
      while(true){ g0=__hip_atomic_load(gp,RLX_AGENT); if(__all((unsigned)(g0>>32)==ep)||++spins>SPIN_CAP) break; __builtin_amdgcn_s_sleep(1); }
      DBG(5,spins>SPIN_CAP);
      float s=sum16(__uint_as_float((unsigned)g0)); s=(rl(s,0)+rl(s,16))+cs+bf2f(bout[0]);
      if(spins>SPIN_CAP) s=__uint_as_float(0x7fc00000u);
      if(lane==0) out[wg*4+q]=(u16)cvtpk(s,0.f);
    }
    STAMP(7);
  } else {
    const int wm=w>>1, wn=w&1;
    u64 ts_[8]={0,0,0,0,0,0,0,0};
    if(tid<8) ldsw<unsigned>(L_CNT+tid*4,0u);
    const rsrc_t rsW=mkrsrc(ws,W_END);
    const int i=4*(wg&7)+w, h=lane>>5, c=lane&31;
    u32x4 gt0,gt1,s0,s1,o3; unsigned o2,o1,v1[6],bb[7],ww[7];
    const i32x4 srdO=mksrd(wout,156);
    if(wg<16){
      const i32x4 srdC=mksrd(wc,3*17576*2), srdG=mksrd(g_gtab.v,2048);
      if(wg<8){
        for(int k=w;k<35;k+=4) dma(L_F3+k*1024,lane*16,srdC,2*35152+k*1024);
        const unsigned so=(lane>>2)*1352+(lane&3)*16;
        ld4(s0,i<26?so:OOB,srdC,35152+i*52); ld4(s1,(i<26&&lane<40)?so:OOB,srdC,35152+16*1352+i*52);
        ld4(o3,104+(lane>>4)*16,srdO,0); ldh(o2,52+2*(c<26?c:0),srdO,0);
      } else {
        #pragma unroll
        for(int k=0;k<6;k++) ld1(v1[k],lane*4+k*256,srdC,(i<26?i:0)*1352);
      }
      ld4(gt0,lane*32,srdG,0); ld4(gt1,lane*32+16,srdG,0);
    }
    { const i32x4 sb=mksrd(b1,DIN*2), sw=mksrd(w2,DIN*2); const unsigned vo=(lane&15)*2;
      #pragma unroll
      for(int n=0;n<7;n++){ const unsigned o=(nt*208+(n<6?wn*112+n*16:96))*2; ldh(bb[n],vo,sb,o); ldh(ww[n],vo,sw,o); } }
    ldh(o1,c<26?c*2:OOB,srdO,0);
    if(wg<16) barl();
    TS(0);
    unsigned aA0,aA1,aB0,aB1,aB6;
    { int row=wm*64+(lane&15); int s=(row>>1)&7;
      aA0=row*128+(((4*wn+(lane>>4))^s)*16); aA1=aA0^64;
      int trrow=4*(lane>>4)+((lane&15)>>2);
      unsigned b=L_W+trrow*WPITCH+(lane&3)*8;
      aB0=b+32*wn*WPITCH+wn*224; aB1=b+32*(wn^1)*WPITCH+wn*224; aB6=b+32*wn*WPITCH+192; }
    asm volatile("":"+v"(aA0),"+v"(aA1),"+v"(aB0),"+v"(aB1),"+v"(aB6));
    f32x4 acc[4][7]; float w2f[7];
    #pragma unroll
    for(int n=0;n<7;n++){ acc[2][n]=f32x4{0,0,0,0}; acc[3][n]=f32x4{0,0,0,0}; pin(acc[2][n]); pin(acc[3][n]); }
    waitv<0>();
    TS(1);
    if(wg<8){
      pin(s0); pin(s1); pin(o2);
      ldsw<u32x4>(L_FA+w*2048+lane*16,s0); ldsw<u32x4>(L_FA+w*2048+1024+lane*16,s1);
      if(w==0){ if(lane<48) ldsw<u32x4>(L_FP+(lane>=24?2048:0)+26*64+(lane>=24?lane-24:lane)*16,u32x4{0,0,0,0});
                if(c<26) ldsw<u16>(L_FP+h*2048+c*64+52,h?(u16)0:(u16)o2); }
      lds_arrive(L_CNT);
    }
    pin(o1); ldsw<float>(L_O1+c*4,bf2f(o1));
    #pragma unroll
    for(int n=0;n<7;n++){ pin(bb[n]); pin(ww[n]); float b=bf2f(bb[n]); w2f[n]=bf2f(ww[n]);
      float b0=(n<6||wn==0)?b:0.f, b1v=(n<6||wn==1)?b:0.f;
      acc[0][n]=f32x4{b0,b0,b0,b0}; acc[1][n]=f32x4{b1v,b1v,b1v,b1v};
      asm volatile("":"+v"(acc[0][n]),"+v"(acc[1][n]),"+v"(w2f[n])); }
    if(wg<16){
      const unsigned dm=L_FD+w*4352;
      pin(gt0); pin(gt1);
      if(wg<8){
        pin(o3);
        TS(2);
        { bool to=lds_wait(L_CNT,4); DBG(0,to); }
        TS(3);
        { const int g=lane>>4, qq=(lane&15)>>2; int r0=8*g+qq, r1=r0+4; r0=r0<26?r0:25; r1=r1<26?r1:25;
          const unsigned t0=L_F3+r0*1352+(lane&3)*8+w*32, t1=L_F3+r1*1352+(lane&3)*8+w*32;
          f32x16 d={};
          #pragma unroll
          for(int k=0;k<11;k++){ f32x4 z={0,0,0,0}; d[k]=MFMA16(BC8(o3),cat(ldT(t0+k*128),ldT(t1+k*128)),z)[0]; }
          #pragma unroll
          for(int rd=0;rd<3;rd++){
            float pa=(g&1)?d[4*rd+1]:d[4*rd], pb=(g&1)?d[4*rd+3]:d[4*rd+2]; pin(pa); pin(pb); float pv=(g&2)?pb:pa;
            int e=16*(w+4*(rd*4+g))+(lane&15);
            int gi=e/26; unsigned hi=cvtpk(pv,0.f), lo=cvtpk(pv-bflo(hi),0.f);
            unsigned a=L_FP+gi*64+(e-gi*26)*2; a=e<676? a : L_FP+62;
            ldsw<u16>(a,(u16)hi); ldsw<u16>(a+2048,(u16)lo);
          } }
        TS(4);
        { bool to=lds_sync(L_CNT+4,4); DBG(1,to); }
        f32x16 D={};
        { const unsigned t=(8*h+((lane&15)>>2))*64+((lane>>4)&1)*32+(lane&3)*8;
          #pragma unroll
          for(int s=0;s<2;s++){
            bf16x8 af=cat(ldT(L_FA+w*2048+t+s*1024),ldT(L_FA+w*2048+t+s*1024+256));
            D=MFMA32(af,cat(ldT(L_FP+t+s*1024),ldT(L_FP+t+s*1024+256)),D);
            D=MFMA32(af,cat(ldT(L_FP+2048+t+s*1024),ldT(L_FP+2048+t+s*1024+256)),D);
          } }
        #pragma unroll
        for(int r=0;r<16;r++) ldsw<float>(dm+(((r&3)+8*(r>>2)+4*h)*33+c)*4,D[r]);
      } else {
        #pragma unroll
        for(int k=0;k<6;k++){ pin(v1[k]); int e=lane+64*k; int a=e/13,b=2*(e-13*a); ldsw<float>(dm+(a*33+b)*4,bflo(v1[k])); ldsw<float>(dm+(a*33+b+1)*4,bfhi(v1[k])); }
      }
      ldsw<float>(dm+1056*4,0.f);
      TS(5);
      { const int hh=lane&1, s=lane>>1; float v[8];
        #pragma unroll
        for(int k=0;k<8;k++){ unsigned o=k<4?gt0[k]:gt1[k-4]; v[k]=ldsr<float>(dm+(o&0xffff))+ldsr<float>(dm+(o>>16)); }
        u32x4 o; o[0]=cvtpk(v[0],v[1]); o[1]=cvtpk(v[2],v[3]); o[2]=cvtpk(v[4],v[5]); o[3]=cvtpk(v[6],v[7]);
        if(i>=26) o=u32x4{0,0,0,0};
        int fq= wg<8? (s<23? 22+s : 23+s) : (s<22? s : 45);
        bool on= s<23 || (s<25&&wg<8);
        __builtin_amdgcn_raw_buffer_store_b128(o,rsW,on?W_FRAG+fq*1024+(hh*32+i)*16:OOB,0,SC1);
      }
    }
    TS(6);
    bf16x8 A0[4]; s16x4 B0[14];
    TS(7);
    wait_ep();
    barl();
    STAMP(1);
    #pragma unroll
    for(int k=0;k<2;k++) B0[k]=ldT(aB0+k*16*WPITCH);
    #pragma unroll
    for(int m=0;m<4;m++) A0[m]=ldsr<bf16x8>(aA0+m*2048);
    #pragma unroll
    for(int k=2;k<12;k++) B0[k]=ldT(aB0+(k>>1)*32+(k&1)*16*WPITCH);
    B0[12]=ldT(aB6); B0[13]=ldT(aB6+16*WPITCH);
    gemm_stage<0,false>(acc,A0,B0,aA0,aA1,aB0,aB1,aB6);
    waitv<0>();
    STAMP(7);
    const unsigned pw=(wg^1)*4+w;
    #pragma nounroll
    for(int it=0;it<8;it++){
      gemm_stage<1,false>(acc,A0,B0,aA0,aA1,aB0,aB1,aB6);
      gemm_stage<2,false>(acc,A0,B0,aA0,aA1,aB0,aB1,aB6);
      gemm_stage<0,false>(acc,A0,B0,aA0,aA1,aB0,aB1,aB6);
    }
    gemm_stage<1,true>(acc,A0,B0,aA0,aA1,aB0,aB1,aB6);
    asm volatile("s_nop 7\n\ts_nop 7":::"memory"); SB0();
    STAMP(2);
    #pragma unroll
    for(int n=0;n<7;n++){
      u32x4 o; o[0]=cvtpk(acc[2][n][0],acc[2][n][1]); o[1]=cvtpk(acc[2][n][2],acc[2][n][3]); o[2]=cvtpk(acc[3][n][0],acc[3][n][1]); o[3]=cvtpk(acc[3][n][2],acc[3][n][3]);
      #pragma unroll
      for(int k=0;k<4;k++) o[k]=o[k]<0xfffffffeu?o[k]:0xfffffffeu;
      __builtin_amdgcn_raw_buffer_store_b128(o,rsW,lane*16+n*1024,W_XBUF+pw*7168,SC1);
    }
    ldsw<f32x4>(L_B6+w*1024+lane*16, wn?acc[0][6]:acc[1][6]);
    STAMP(3);
    u32x4 pr[8];
    auto fetch=[&](){
      #pragma unroll
      for(int n=0;n<7;n++) pr[n]=__builtin_amdgcn_raw_buffer_load_b128(rsW,lane*16+n*1024,W_XBUF+(wg*4+w)*7168,SC1);
      pr[7]=__builtin_amdgcn_raw_buffer_load_b128(rsW,lane*16+6*1024,W_XBUF+(wg*4+(w^1))*7168,SC1); };
    fetch();
    { bool to=lds_sync(L_CNT+8+wm*4,2); DBG(2,to); }
    f32x4 o6=ldsr<f32x4>(L_B6+(w^1)*1024+lane*16);
    { unsigned spins=0;
      while(true){ unsigned m=0;
        #pragma unroll
        for(int n=0;n<8;n++) m=max(max(m,max(pr[n][0],pr[n][1])),max(pr[n][2],pr[n][3]));
        if(__all(m!=~0u)||++spins>SPIN_CAP) break;
        __builtin_amdgcn_s_sleep(2); fetch(); }
      DBG(6,spins>SPIN_CAP); }
    STAMP(4);
    float rs[8]={0,0,0,0,0,0,0,0};
    #pragma unroll
    for(int n=0;n<6;n++){
      #pragma unroll
      for(int m=0;m<2;m++){
        rs[4*m+0]+=gelu_w(acc[m][n][0]+bflo(pr[n][2*m]),w2f[n]);   rs[4*m+1]+=gelu_w(acc[m][n][1]+bfhi(pr[n][2*m]),w2f[n]);
        rs[4*m+2]+=gelu_w(acc[m][n][2]+bflo(pr[n][2*m+1]),w2f[n]); rs[4*m+3]+=gelu_w(acc[m][n][3]+bfhi(pr[n][2*m+1]),w2f[n]);
      } }
    { f32x4 k6=wn?acc[1][6]:acc[0][6];
      unsigned pa0=wn?pr[6][2]:pr[6][0], pa1=wn?pr[6][3]:pr[6][1], pb0=wn?pr[7][2]:pr[7][0], pb1=wn?pr[7][3]:pr[7][1];
      float g0=gelu_w(k6[0]+o6[0]+bflo(pa0)+bflo(pb0),w2f[6]), g1=gelu_w(k6[1]+o6[1]+bfhi(pa0)+bfhi(pb0),w2f[6]);
      float g2=gelu_w(k6[2]+o6[2]+bflo(pa1)+bflo(pb1),w2f[6]), g3=gelu_w(k6[3]+o6[3]+bfhi(pa1)+bfhi(pb1),w2f[6]);
      rs[0]+=wn?0.f:g0; rs[1]+=wn?0.f:g1; rs[2]+=wn?0.f:g2; rs[3]+=wn?0.f:g3; rs[4]+=wn?g0:0.f; rs[5]+=wn?g1:0.f; rs[6]+=wn?g2:0.f; rs[7]+=wn?g3:0.f; }
    #pragma unroll
    for(int k=0;k<8;k++) rs[k]=sum16(rs[k]);
    { const int cc=lane&15; float v=rs[0];
      #pragma unroll
      for(int k=1;k<8;k++) v=(cc==k)?rs[k]:v;
      int owner=mt*32+wm*16+(2*ks+(cc>>2))*4+(lane>>4);
      if(cc<8) __hip_atomic_store((GAS u64*)(ws+W_GRAN+owner*1024+(nt*2+wn)*32+(cc&3)*8),((u64)ep<<32)|__float_as_uint(v),RLX_AGENT);
    }
    STAMP(5);
    TSOUT(8);TSOUT(9);TSOUT(10);TSOUT(11);TSOUT(12);TSOUT(13);TSOUT(14);TSOUT(15);
    if(wg==0 && w==0){
      unsigned spins=0; const GAS unsigned* sp=(const GAS unsigned*)(ws+W_STARTED)+lane;
      while(true){ bool ok=true;
        #pragma unroll
        for(int k=0;k<4;k++) ok&=__hip_atomic_load(sp+64*k,RLX_AGENT)==ep;
        if(__all(ok)||++spins>SPIN_CAP) break; __builtin_amdgcn_s_sleep(1); }
      if(lane==0) __hip_atomic_store((GAS unsigned*)(ws+W_EPOCH),ep,RLX_AGENT);
    }
  }
}
extern "C" void kernel_launch(void* const* d_in,const int* in_sizes,int n_in,void* d_out,int out_size,void* d_ws,size_t ws_size,hipStream_t stream){
  (void)hipFuncSetAttribute((const void*)k_main,hipFuncAttributeMaxDynamicSharedMemorySize,L_TOTAL);
  k_main<<<256,512,L_TOTAL,stream>>>((const int*)d_in[0],(const u16*)d_in[1],(const u16*)d_in[2],(const u16*)d_in[3],(const u16*)d_in[4],(const u16*)d_in[5],(const u16*)d_in[6],(const u16*)d_in[7],(u16*)d_out,(char*)d_ws);
}
```

```cpp
#include <hip/hip_runtime.h>
#include <hip/hip_bf16.h>
typedef unsigned short u16;
typedef unsigned long long u64;
typedef int i32x4 __attribute__((ext_vector_type(4)));
typedef unsigned u32x4 __attribute__((ext_vector_type(4)));
typedef unsigned u32x2 __attribute__((ext_vector_type(2)));
typedef short s16x4 __attribute__((ext_vector_type(4)));
typedef __bf16 bf16x8 __attribute__((ext_vector_type(8)));
typedef __bf16 bf16x2 __attribute__((ext_vector_type(2)));
typedef float f32x2 __attribute__((ext_vector_type(2)));
typedef float f32x4 __attribute__((ext_vector_type(4)));
typedef float f32x16 __attribute__((ext_vector_type(16)));
#define DI __device__ __forceinline__
#define LAS __attribute__((address_space(3)))
#define GAS __attribute__((address_space(1)))
#define STAMPS 0
#define CODEPF 22528
#define TLBW 1
#define SPINLOG 18
#define NOCLR 0
#define NOX0 0
#define NOCIN 0
#define PFD 2
#define PFP 2
#define THR 63
#define STR_(x) #x
#define STR(x) STR_(x)
#define THRW "s_waitcnt vmcnt(" STR(THR) ")\n\t"
#define PRIO 0
#define CPRIO 0
#define LPRIO 1
constexpr int NB=1024,NF=26,ND=128,DIN=3328,ROWB=DIN*2;
constexpr int ASLOT=16384, WSLOT=26624, WPITCH=416, L_W=3*ASLOT;
constexpr int L_A2=2*ASLOT, L_W2=L_W+2*WSLOT;
constexpr int L_CW=L_W+3*WSLOT, L_IDX=L_CW+26624, L_MISC=L_IDX+7168, L_TOTAL=163840;
constexpr int L_X0=L_CW, L_CNT=L_MISC, L_FL=L_MISC+64, L_O1=L_MISC+128, L_PFD=L_MISC+768;
constexpr int L_B6=ASLOT;
constexpr int L_F3=L_W2, L_FA=L_W2+35840, L_FP=L_FA+8192, L_FD=L_W2;
static_assert(L_FP+4096<=L_IDX && L_MISC+1024==L_TOTAL,"lds map");
constexpr unsigned W_EPOCH=0, W_STARTED=256, W_FFLAG=2048, W_XFLAG=4096, W_FRAG=4096+65536, W_GRAN=W_FRAG+49152, W_XBUF=W_GRAN+262144, W_END=W_XBUF+256*4*10240;
constexpr unsigned SPIN_CAP=1u<<SPINLOG, OOB=0x7ffffff0u;

DI float bf2f(unsigned x){ return __uint_as_float(x<<16); }
DI float bflo(unsigned x){ return __uint_as_float(x<<16); }
DI float bfhi(unsigned x){ return __uint_as_float(x&0xffff0000u); }
DI unsigned cvtpk(float lo,float hi){ f32x2 v={lo,hi}; bf16x2 b=__builtin_convertvector(v,bf16x2); return __builtin_bit_cast(unsigned,b); }
DI void dma(unsigned m0v, unsigned voff, i32x4 srd, unsigned soff){
  asm volatile("s_mov_b32 m0, %0\n\t" THRW "buffer_load_dwordx4 %1, %2, %3 offen lds" :: "s"(m0v), "v"(voff), "s"(srd), "s"(soff) : "memory");
}
DI void dma_sc1(unsigned m0v, unsigned voff, i32x4 srd, unsigned soff){
  asm volatile("s_mov_b32 m0, %0\n\ts_nop 0\n\tbuffer_load_dwordx4 %1, %2, %3 offen sc1 lds" :: "s"(m0v), "v"(voff), "s"(srd), "s"(soff) : "memory");
}
DI void dma_lo32(unsigned m0v, unsigned voff, i32x4 srd, unsigned soff){
  asm volatile("s_mov_b32 m0, %0\n\ts_mov_b64 exec, %4\n\tbuffer_load_dwordx4 %1, %2, %3 offen lds\n\ts_mov_b64 exec, -1" :: "s"(m0v), "v"(voff), "s"(srd), "s"(soff), "s"(0xFFFFFFFFull) : "memory");
}
DI void dma_lo16(unsigned m0v, unsigned voff, i32x4 srd, unsigned soff){
  asm volatile("s_mov_b32 m0, %0\n\ts_mov_b64 exec, 0xffff\n\tbuffer_load_dwordx4 %1, %2, %3 offen lds\n\ts_mov_b64 exec, -1" :: "s"(m0v), "v"(voff), "s"(srd), "s"(soff) : "memory");
}
constexpr u64 MASK52=0x000FFFFFFFFFFFFFull;
template<int I> DI void dmaW(unsigned m0v, unsigned voff, i32x4 srd, unsigned so){
  unsigned t;
  asm volatile("s_add_u32 m0, %1, %6\n\ts_add_u32 %0, %4, %7\n\ts_mov_b64 exec, %5\n\t" THRW "buffer_load_dwordx4 %2, %3, %0 offen lds\n\ts_mov_b64 exec, -1"
    : "=&s"(t) : "s"(m0v), "v"(voff), "s"(srd), "s"(so), "s"(MASK52), "n"(832*I), "n"((8*(I>>1)+2*(I&1))*ROWB) : "memory","scc");
}
DI void dmaW8(unsigned m0v, unsigned voff, i32x4 srd, unsigned so){
  dmaW<0>(m0v,voff,srd,so); dmaW<1>(m0v,voff,srd,so); dmaW<2>(m0v,voff,srd,so); dmaW<3>(m0v,voff,srd,so);
  dmaW<4>(m0v,voff,srd,so); dmaW<5>(m0v,voff,srd,so); dmaW<6>(m0v,voff,srd,so); dmaW<7>(m0v,voff,srd,so);
}
DI void pfload(unsigned voff, i32x4 srd, unsigned soff){
  asm volatile("s_mov_b32 m0, %3\n\ts_nop 0\n\tbuffer_load_dword %0, %1, %2 offen lds" :: "v"(voff), "s"(srd), "s"(soff), "n"(L_PFD) : "memory"); }
DI void ld4_sc1(u32x4& d,unsigned voff,i32x4 srd,unsigned soff){ asm volatile("buffer_load_dwordx4 %0, %1, %2, %3 offen sc1":"=v"(d):"v"(voff),"s"(srd),"s"(soff):"memory"); }
DI void st4_sc1(u32x4 d,unsigned voff,i32x4 srd,unsigned soff){ asm volatile("buffer_store_dwordx4 %0, %1, %2, %3 offen sc1\n\ts_nop 1"::"v"(d),"v"(voff),"s"(srd),"s"(soff):"memory"); }
DI void ld1_sc1(unsigned& d,unsigned voff,i32x4 srd,unsigned soff){ asm volatile("buffer_load_dword %0, %1, %2, %3 offen sc1":"=v"(d):"v"(voff),"s"(srd),"s"(soff):"memory"); }
DI void ld4(u32x4& d,unsigned voff,i32x4 srd,unsigned soff){ asm volatile("buffer_load_dwordx4 %0, %1, %2, %3 offen":"=v"(d):"v"(voff),"s"(srd),"s"(soff):"memory"); }
DI void ldh(unsigned& d,unsigned voff,i32x4 srd,unsigned soff){ asm volatile("buffer_load_ushort %0, %1, %2, %3 offen":"=v"(d):"v"(voff),"s"(srd),"s"(soff):"memory"); }
DI void ld1(unsigned& d,unsigned voff,i32x4 srd,unsigned soff){ asm volatile("buffer_load_dword %0, %1, %2, %3 offen":"=v"(d):"v"(voff),"s"(srd),"s"(soff):"memory"); }
template<class T> DI void pin(T& x){ asm volatile("":"+v"(x)); }
template<int N> DI void waitv(){ asm volatile("s_waitcnt vmcnt(%0)"::"n"(N):"memory"); }
DI void bar(){ asm volatile("s_barrier":::"memory"); }
DI void barl(){ asm volatile("s_waitcnt lgkmcnt(0)\n\ts_barrier":::"memory"); }
DI i32x4 mksrd(const void* p, unsigned bytes){
  u64 a=(u64)p; i32x4 s;
  s[0]=__builtin_amdgcn_readfirstlane((unsigned)a); s[1]=__builtin_amdgcn_readfirstlane((unsigned)(a>>32)&0xffff); s[2]=bytes; s[3]=0x20000; return s;
}
typedef __amdgpu_buffer_rsrc_t rsrc_t;
DI rsrc_t mkrsrc(const void* p, unsigned bytes){ return __builtin_amdgcn_make_buffer_rsrc((void*)p,0,bytes,0x20000); }
#define SC1 16
#define RLX_AGENT __ATOMIC_RELAXED, __HIP_MEMORY_SCOPE_AGENT
#define SB0() __builtin_amdgcn_sched_barrier(0)
#define STAMP(i)
#define TS(i)
#define TSOUT(i)
#define DBG(code,cond)
template<class T> DI T ldsr(unsigned a){ return *(const LAS T*)(size_t)a; }
template<class T> DI void ldsw(unsigned a,T v){ *(LAS T*)(size_t)a=v; }
DI s16x4 ldT(unsigned a){ return __builtin_amdgcn_ds_read_tr16_b64_v4i16((LAS s16x4*)(size_t)a); }
DI bf16x8 cat(s16x4 lo,s16x4 hi){ return __builtin_bit_cast(bf16x8,__builtin_shufflevector(lo,hi,0,1,2,3,4,5,6,7)); }
#define MFMA16(a,b,c) __builtin_amdgcn_mfma_f32_16x16x32_bf16(a,b,c,0,0,0)
#define MFMA32(a,b,c) __builtin_amdgcn_mfma_f32_32x32x16_bf16(a,b,c,0,0,0)
#define BC8(x) __builtin_bit_cast(bf16x8,x)
template<int CTRL> DI float dppadd(float v){ return v+__int_as_float(__builtin_amdgcn_update_dpp(0,__float_as_int(v),CTRL,0xf,0xf,true)); }
DI float sum16(float v){ v=dppadd<0xB1>(v); v=dppadd<0x4E>(v); v=dppadd<0x141>(v); v=dppadd<0x140>(v); return v; }
DI float rl(float v,int l){ return __int_as_float(__builtin_amdgcn_readlane(__float_as_int(v),l)); }
DI unsigned lds_inc(unsigned addr){
  unsigned old,r;
  asm volatile("s_mov_b64 exec, 1\n\tds_add_rtn_u32 %0, %2, %3\n\ts_mov_b64 exec, -1\n\ts_waitcnt lgkmcnt(0)\n\tv_readfirstlane_b32 %1, %0":"=&v"(old),"=s"(r):"v"(addr),"v"(1u):"memory");
  return r;
}
DI unsigned lds_peek(unsigned addr){ unsigned v,r;
  asm volatile("ds_read_b32 %0, %2\n\ts_waitcnt lgkmcnt(0)\n\tv_readfirstlane_b32 %1, %0":"=&v"(v),"=s"(r):"v"(addr):"memory"); return r; }
DI void lds_arrive(unsigned cnt){ asm volatile("s_waitcnt lgkmcnt(0)\n\ts_mov_b64 exec, 1\n\tds_add_u32 %0, %1\n\ts_mov_b64 exec, -1"::"v"(cnt),"v"(1u):"memory"); }
DI bool lds_wait(unsigned cnt,unsigned n){ unsigned spins=0; while(lds_peek(cnt)<n && ++spins<=SPIN_CAP) __builtin_amdgcn_s_sleep(0); return spins>SPIN_CAP; }
DI void lds_flag(unsigned addr,unsigned tag){ asm volatile("s_waitcnt lgkmcnt(0)":::"memory"); ldsw<unsigned>(addr,tag); }
DI bool lds_flags(unsigned addr,unsigned mask,unsigned tag,int lane){ unsigned spins=0, a=addr+(lane&mask)*4, v;
  while(true){ asm volatile("ds_read_b32 %0, %1\n\ts_waitcnt lgkmcnt(0)":"=v"(v):"v"(a):"memory"); if(__all(v==tag)||++spins>SPIN_CAP) break; }
  return spins>SPIN_CAP; }
DI bool lds_sync(unsigned cnt,unsigned n){
  asm volatile("s_waitcnt lgkmcnt(0)":::"memory");
  unsigned spins=0, v=lds_inc(cnt)+1u;
  while(v<n && ++spins<=SPIN_CAP){ __builtin_amdgcn_s_sleep(0); v=lds_peek(cnt); }
  return spins>SPIN_CAP;
}
DI void mfma16_acc(f32x4& c,bf16x8 a,bf16x8 b){ asm volatile("v_mfma_f32_16x16x32_bf16 %0, %1, %2, %0":"+v"(c):"v"(a),"v"(b)); }

template<int S,bool LAST> DI void gemm_stage(f32x4 (&acc)[4][7],bf16x8 (&A0)[4],s16x4 (&B0)[14],unsigned aA0,unsigned aA1,unsigned aB0,unsigned aB1,unsigned aB6){
  constexpr int SN=(S+1)%3, HI=16*WPITCH;
  bf16x8 A1[4]; s16x4 B1[12];
  #pragma unroll
  for(int i=0;i<28;i++){ const int n=i>>2,m=i&3;
    mfma16_acc(acc[m][n],A0[m],cat(B0[2*n],B0[2*n+1])); SB0();
    if(i<2) B1[i]=ldT(aB1+S*WSLOT+(i&1)*HI);
    else if(i<6) A1[i-2]=ldsr<bf16x8>(aA1+S*ASLOT+(i-2)*2048);
    else if(i<16){ const int k=i-4; B1[k]=ldT(aB1+S*WSLOT+(k>>1)*32+(k&1)*HI); }
    SB0();
    if(i==22){ barl(); SB0(); }
  }
  #pragma unroll
  for(int i=0;i<24;i++){ const int n=i>>2,m=i&3;
    mfma16_acc(acc[m][n],A1[m],cat(B1[2*n],B1[2*n+1])); SB0();
    if(!LAST){
      if(i<2) B0[i]=ldT(aB0+SN*WSLOT+(i&1)*HI);
      else if(i<6) A0[i-2]=ldsr<bf16x8>(aA0+SN*ASLOT+(i-2)*2048);
      else if(i<16){ const int k=i-4; B0[k]=ldT(aB0+SN*WSLOT+(k>>1)*32+(k&1)*HI); }
      else if(i<18) B0[i-4]=ldT(aB6+SN*WSLOT+(i&1)*HI);
    }
    SB0();
  }
}

template<int T> struct Slot { static constexpr int u=T>>1, a=T<182?u%13:0, b=T<182?(u%13+u/13)%13:0; };
DI void mfma32_acc(f32x16& c,u32x4 a,u32x4 b){ asm volatile("v_mfma_f32_32x32x16_bf16 %0, %1, %2, %0":"+v"(c):"v"(a),"v"(b)); }
DI void zq(unsigned& o0,unsigned& o1,float a0,f32x2 b0,float a1,f32x2 b1){
  float t0,t1,t2,t3;
  asm volatile("v_mul_f32_e32 %2, %6, %7\n\tv_mul_f32_e32 %3, %6, %8\n\tv_mul_f32_e32 %4, %9, %10\n\tv_mul_f32_e32 %5, %9, %11\n\tv_cvt_pk_bf16_f32 %0, %2, %3\n\tv_cvt_pk_bf16_f32 %1, %4, %5"
    :"=&v"(o0),"=&v"(o1),"=&v"(t0),"=&v"(t1),"=&v"(t2),"=&v"(t3):"v"(a0),"v"(b0[0]),"v"(b0[1]),"v"(a1),"v"(b1[0]),"v"(b1[1]));
}
DI void zl(unsigned& o0,unsigned& o1,float a,float b,float c,float d){ asm volatile("v_cvt_pk_bf16_f32 %0, %2, %3\n\tv_cvt_pk_bf16_f32 %1, %4, %5":"=&v"(o0),"=&v"(o1):"v"(a),"v"(b),"v"(c),"v"(d)); }
template<int S,int H> DI void zhalf(const f32x2 (&R)[13],u32x4& z){
  unsigned o0,o1;
  if constexpr(S<23){ using P=Slot<8*S+4*H>; using Q=Slot<8*S+4*H+2>; zq(o0,o1,R[P::a][0],R[P::b],R[Q::a][0],R[Q::b]); }
  else if constexpr(S==23) zl(o0,o1,R[4*H][0],R[4*H+1][0],R[4*H+2][0],R[4*H+3][0]);
  else if constexpr(S==24){ if constexpr(H==0) zl(o0,o1,R[8][0],R[9][0],R[10][0],R[11][0]); else zl(o0,o1,R[12][0],R[12][0],R[12][0],R[12][0]); }
  if constexpr(S<25){ z[2*H]=o0; z[2*H+1]=o1; }
}
__host__ __device__ constexpr unsigned wfrag_addr(int q){ return q<26? L_W2+q*1024 : q<42? L_A2+(q-26)*1024 : L_IDX+(q-42)*1024; }
DI void mfma32_0(f32x16& c,u32x4 a,u32x4 b){ asm volatile("v_mfma_f32_32x32x16_bf16 %0, %1, %2, 0":"=&v"(c):"v"(a),"v"(b)); }
DI float fma_s(float a,float b,float c){ float r; asm("v_fma_f32 %0, %1, %2, %3":"=v"(r):"v"(a),"v"(b),"v"(c)); return r; }
struct Cin { f32x2 R0[13],R1[13]; f32x16 x0,v0,x1,v1; u32x4 z0[2],z1[2],wa[2],wb[2]; unsigned la,lb; };
template<int Q> DI u32x4 wfrag(const Cin& c){ if constexpr(Q>=26&&Q<42) return ldsr<u32x4>(c.la+(wfrag_addr(Q)-L_A2)); else return ldsr<u32x4>(c.lb+(wfrag_addr(Q)-L_W2)); }
template<int S> DI void cin_step(Cin& c){
  if constexpr(S==0) mfma32_0(c.x0,c.wa[0],c.z0[0]); else if constexpr(S<23) mfma32_acc(c.x0,c.wa[S&1],c.z0[S&1]);
  SB0(); zhalf<S+1,0>(c.R0,c.z0[(S+1)&1]); SB0();
  mfma32_acc(c.v0,c.wb[S&1],c.z0[S&1]);
  SB0(); zhalf<S+1,1>(c.R0,c.z0[(S+1)&1]); SB0();
  if constexpr(S==0) mfma32_0(c.x1,c.wa[0],c.z1[0]); else if constexpr(S<23) mfma32_acc(c.x1,c.wa[S&1],c.z1[S&1]);
  SB0(); if constexpr(S+2<23) c.wa[S&1]=wfrag<2*S+4>(c);
  zhalf<S+1,0>(c.R1,c.z1[(S+1)&1]); SB0();
  mfma32_acc(c.v1,c.wb[S&1],c.z1[S&1]);
  SB0(); if constexpr(S+2<23) c.wb[S&1]=wfrag<2*S+5>(c); else if constexpr(S+2<25) c.wb[S&1]=wfrag<23+S+2>(c);
  zhalf<S+1,1>(c.R1,c.z1[(S+1)&1]); SB0();
}
template<int S> DI void cin_run(Cin& c){ if constexpr(S<25){ cin_step<S>(c); cin_run<S+1>(c); } }

struct GTab { u16 v[64*16]; };
constexpr GTab make_gtab(){ GTab t{};
  for(int c=0;c<64;c++) for(int k=0;k<8;k++){ int hh=c&1,s=c>>1,o0=1056,o1=1056;
    if(s<23){ int tt=8*s+k,u=tt>>1,dl=u/13,a=u%13,b=(a+dl)%13+13*(k&1),pa=a+13*hh,pb=(b+13*hh)%26;
      if(tt<182 && !(hh&&(k&1)&&dl==0)){ o0=pa*33+pb; if(pa!=pb) o1=pb*33+pa; } }
    else if(s<25){ int a=8*(s-23)+k; if(a<13) o0=(a+13*hh)*33+26; }
    t.v[c*16+2*k]=(u16)(o0*4); t.v[c*16+2*k+1]=(u16)(o1*4); }
  return t; }
__device__ const GTab g_gtab=make_gtab();
DI float gelu_w(float x,float w2){
  float t=x*(-2.302208198f-0.1029432397f*x*x);
  float e=__builtin_amdgcn_exp2f(t);
  return x*__builtin_amdgcn_rcpf(1.f+e)*w2;
}

struct Ldr { i32x4 srdT,srdW; unsigned wv,wl,al,t4,asw0,asw1,so,sa,sw,st; };
DI void ldr_next(Ldr& L){ L.sw=(L.sa==2*ASLOT)?0:L.sw+WSLOT; L.sa=(L.sa==2*ASLOT)?0:L.sa+ASLOT; L.so+=64*ROWB; L.st+=1; }
template<int P> DI void dmaA(Ldr& L,const u32x4& ro){ dma(L.sa+L.al+P*1024,ro[P]+((P&1)?L.asw1:L.asw0),L.srdT,(L.st&1)*128); }
DI void ldr_issue(Ldr& L){
  u32x4 ro=ldsr<u32x4>(L.t4+(L.st>>1)*512);
  dmaW8(L.sw+L.wl,L.wv,L.srdW,L.so);
  dmaA<0>(L,ro); dmaA<1>(L,ro); dmaA<2>(L,ro); dmaA<3>(L,ro);
  ldr_next(L);
}
__global__ __launch_bounds__(512,2) void k_main(const int* __restrict__ idx,const u16* __restrict__ table,const u16* __restrict__ wc,const u16* __restrict__ wout,
    const u16* __restrict__ w1,const u16* __restrict__ b1,const u16* __restrict__ w2,const u16* __restrict__ bout,u16* __restrict__ out,char* ws){
  extern __shared__ __attribute__((aligned(1024))) char lds[];
  const int tid=threadIdx.x, lane=tid&63, w=__builtin_amdgcn_readfirstlane(tid>>6);
  const int wg=blockIdx.x, xcd=wg&7, j=wg>>3, ks=xcd&1, mt=j&7, nl=j>>3, nt=(xcd>>1)*4+nl;
  if(wg<16){
    u64 pc; asm volatile("s_getpc_b64 %0":"=s"(pc));
    pfload((unsigned)(tid*128),mksrd((const void*)(pc&~255ull),CODEPF),0);
  }
  STAMP(0);
  asm volatile(""::"s"(idx),"s"(table),"s"(wc),"s"(wout),"s"(w1),"s"(b1),"s"(w2),"s"(bout),"s"(out),"s"(ws));
  unsigned ep;
  unsigned ep_raw;
  asm volatile("s_load_dword %0, %1, 0x0":"=s"(ep_raw):"s"(ws):"memory");
  auto wait_ep=[&](){ asm volatile("s_waitcnt lgkmcnt(0)\n\ts_add_u32 %0, %1, 1":"=s"(ep):"s"(ep_raw):"memory","scc"); };
  const i32x4 srdS=mksrd(ws,W_END);
  if(wg<8){
    const i32x4 srdC=mksrd(wc,3*17576*2);
    #pragma unroll
    for(int k=0;k<5;k++) if(k<4||w<3) dma(L_F3+(w+8*k)*1024,lane*16,srdC,2*35152+(w+8*k)*1024);
  }
  if(__builtin_expect(w>=4,1)){
    const int q=w-4;
    Ldr L; L.srdT=mksrd(table,100000u*256u); L.srdW=mksrd(w1,(unsigned)DIN*ROWB);
    const i32x4 srdI=mksrd(idx,NB*NF*4);
    const int r8=lane>>3, c8=lane&7, h=lane>>5;
    u32x2 iv[4]; unsigned i0[4], ix;
    const unsigned irow=((mt*128+((32*q)^(32*ks))+r8)*NF+ks*13)*4;
    #pragma unroll
    for(int p=0;p<4;p++) asm volatile("buffer_load_dword %0, %1, %2, 0 offen offset:%3":"=v"(i0[p]):"v"(irow),"s"(srdI),"n"(p*8*NF*4):"memory");
    #pragma unroll
    for(int p=0;p<4;p++) asm volatile("buffer_load_dwordx2 %0, %1, %2, 0 offen offset:%3":"=v"(iv[p]):"v"(irow+8*c8),"s"(srdI),"n"(p*8*NF*4):"memory");
    ld1(ix,(lane<26?lane:0)*4,srdI,(wg*4+q)*NF*4);
    if(q==0) pfload(lane<25? (unsigned)lane<<20 : OOB, L.srdT, 0);
    L.wv=(lane>=26?ROWB+(lane-26)*16:lane*16)+nt*WPITCH;
    L.so=(ks*1664+32*(q>>1)+4*(q&1))*ROWB;
    L.wl=L_W+q*16*WPITCH; L.al=q*4096;
    dmaW8(L.wl,L.wv,L.srdW,L.so);
    const unsigned pfw=(lane<52)? (ks*1664+4*j+lane/13)*ROWB+(xcd>>1)*1664+(lane%13)*128 : OOB;
    const bool doA=PFD && q==nl, doW=PFD && q==((nl+1)&3);
    L.asw0=(c8^((r8>>1)&7))*16; L.asw1=L.asw0^64;
    if(doW){
      #pragma unroll
      for(int f=1;f<=PFD;f++) pfload(pfw,L.srdW,f*128*ROWB);
      waitv<8+PFD>(); } else waitv<8>();
    #pragma unroll
    for(int p=0;p<4;p++){ pin(i0[p]); pin(iv[p]); }
    pin(ix);
    wait_ep();
    if(wg<8) lds_flag(L_FL+w*4,ep);
    #pragma unroll
    for(int p=0;p<4;p++){ i0[p]=(i0[p]<<8)+((p&1)?L.asw1:L.asw0); dma(L.al+p*1024,i0[p],L.srdT,0); }
    dmaW8(WSLOT+L.wl,L.wv,L.srdW,L.so+64*ROWB);
    #pragma unroll
    for(int p=0;p<4;p++) dma(ASLOT+L.al+p*1024,i0[p],L.srdT,128);
    if(c8<7){
      #pragma unroll
      for(int p=0;p<4;p++){ ldsw<unsigned>(L_IDX+((2*c8)*128+q*32+r8*4+p)*4,iv[p][0]<<8); ldsw<unsigned>(L_IDX+((2*c8+1)*128+q*32+r8*4+p)*4,iv[p][1]<<8); }
    }
    L.t4=L_IDX+(q*8+r8)*16; L.sa=2*ASLOT; L.sw=2*WSLOT; L.so+=128*ROWB; L.st=2;
    const unsigned tp=L_IDX+(q*32+((lane>>1)&7)*4+(lane>>4))*4, pfa=(lane&1)*128;
    if(doA){
      #pragma unroll
      for(int f=1;f<=PFD;f++) pfload(ldsr<unsigned>(tp+f*512)+pfa,L.srdT,0);
      waitv<12+PFD>(); } else waitv<12>();
    STAMP(1);
    barl();
    __builtin_amdgcn_s_setprio(LPRIO);
    if(tid==256) __hip_atomic_store((GAS unsigned*)(ws+W_STARTED)+wg,ep,RLX_AGENT);
    auto field=[&](int f){
      ldr_issue(L);
      if((doA||doW) && f>1 && f-1+PFD<13) waitv<13>(); else waitv<12>();
      bar();
      if(f==2 && wg<16 && tid==256) __hip_atomic_store((GAS unsigned*)(ws+W_FFLAG)+wg,ep,RLX_AGENT);
      ldr_issue(L);
      if((doA||doW) && f+PFD<13){
        if(doA) pfload(ldsr<unsigned>(tp+(f+PFD)*512)+pfa,L.srdT,0); else pfload(pfw,L.srdW,(f+PFD)*128*ROWB);
        waitv<13>();
      } else waitv<12>();
      bar(); };
    #pragma nounroll
    for(int f=1;f<12;f++){ if(f==4){ STAMP(9); } if(f==7){ STAMP(10); } if(f==10){ STAMP(11); } field(f); }
    unsigned fl=ep; if(q==0) ld1_sc1(fl,(lane&15)*4,srdS,W_FFLAG);
    #pragma unroll
    for(int i=0;i<7;i++){ unsigned a=(unsigned)__builtin_amdgcn_ds_bpermute((4*i+(lane>>4))*4,(int)ix)*256u+(lane&15)*16;
      if(i<6) dma(L_X0+q*6656+i*1024,a,L.srdT,0); else dma_lo32(L_X0+q*6656+i*1024,a,L.srdT,0); }
    ldr_issue(L); waitv<12>(); pin(fl);
    { unsigned spins=0; while(!__all(fl==ep) && ++spins<=SPIN_CAP){ __builtin_amdgcn_s_sleep(16); ld1_sc1(fl,(lane&15)*4,srdS,W_FFLAG); waitv<0>(); pin(fl); } DBG(3,spins>SPIN_CAP); }
    bar();
    ldr_issue(L); waitv<12>(); bar();
    STAMP(2);
    auto wdma=[&](int i){ const int qq=4*i+q; unsigned a=qq*1024+(qq<26? L_W2 : L_A2-26*1024); a=qq<42? a : qq*1024+(L_IDX-42*1024); dma_sc1(a,lane*16,srdS,W_FRAG+qq*1024); };
    #pragma unroll
    for(int i=0;i<6;i++) wdma(i);
    waitv<6>();
    bar();
    #pragma unroll
    for(int i=6;i<12;i++) wdma(i);
    Cin c; c.la=L_A2+lane*16; c.lb=L_W2+lane*16; pin(c.la); pin(c.lb);
    unsigned raw[26], xa=L_X0+q*6656+(lane&31)*4+h*13*256, xc=L_X0+q*6656+(lane&31)*4+(1-h)*13*256, oa=L_O1+h*16; pin(xa); pin(xc);
    #pragma unroll
    for(int i=0;i<13;i++){ raw[i]=ldsr<unsigned>(xa+i*256); raw[13+i]=ldsr<unsigned>(xc+i*256); }
    const unsigned bo=bout[0];
    waitv<0>();
    bar();
    __builtin_amdgcn_s_setprio(0);
    STAMP(8);
    float s0=0.f,s1=0.f,s2=0.f,s3=0.f;
    #pragma nounroll
    for(int g=0;g<2;g++){
      c.wa[0]=wfrag<0>(c); c.wb[0]=wfrag<1>(c); c.wa[1]=wfrag<2>(c); c.wb[1]=wfrag<3>(c);
      #pragma unroll
      for(int k=0;k<4;k++){ f32x4 o=ldsr<f32x4>(oa+k*32); pin(oa); f32x4 p=ldsr<f32x4>(oa+k*32);
        #pragma unroll
        for(int e=0;e<4;e++){ c.v0[4*k+e]=o[e]; c.v1[4*k+e]=p[e]; } }
      #pragma unroll
      for(int i=0;i<13;i++){ c.R0[i][0]=bflo(raw[i]); c.R1[i][0]=bfhi(raw[i]); c.R0[i][1]=bflo(raw[13+i]); c.R1[i][1]=bfhi(raw[13+i]); }
      #pragma unroll
      for(int i=0;i<13;i++){ raw[i]=ldsr<unsigned>(xa+128+i*256); raw[13+i]=ldsr<unsigned>(xc+128+i*256); }
      zhalf<0,0>(c.R0,c.z0[0]); zhalf<0,1>(c.R0,c.z0[0]); zhalf<0,0>(c.R1,c.z1[0]); zhalf<0,1>(c.R1,c.z1[0]);
      pin(c.v0); pin(c.v1); SB0();
      cin_run<0>(c);
      asm volatile("s_nop 10":"+v"(c.x0),"+v"(c.v0));
      #pragma unroll
      for(int r=0;r<16;r+=2){ s0=fma_s(c.x0[r],c.v0[r],s0); s1=fma_s(c.x0[r+1],c.v0[r+1],s1); }
      asm volatile("":"+v"(c.x1),"+v"(c.v1),"+v"(s0),"+v"(s1));
      #pragma unroll
      for(int r=0;r<16;r+=2){ s2=fma_s(c.x1[r],c.v1[r],s2); s3=fma_s(c.x1[r+1],c.v1[r+1],s3); }
    }
    const GAS u64* gp=(const GAS u64*)(ws+W_GRAN+wg*1024+(lane&31)*32+q*8);
    u64 g0=__hip_atomic_load(gp,RLX_AGENT);
    float cs=sum16((s0+s1)+(s2+s3)); cs=(rl(cs,0)+rl(cs,16))+(rl(cs,32)+rl(cs,48))+bf2f(bo);
    STAMP(6);
    unsigned spins=0;
    while(!__all((unsigned)(g0>>32)==ep) && ++spins<=SPIN_CAP){ __builtin_amdgcn_s_sleep(4); g0=__hip_atomic_load(gp,RLX_AGENT); }
    DBG(5,spins>SPIN_CAP);
    float s=sum16(__uint_as_float((unsigned)g0)); s=(rl(s,0)+rl(s,16))+cs;
    if(spins>SPIN_CAP) s=__uint_as_float(0x7fc00000u);
    if(lane==0) out[wg*4+q]=(u16)cvtpk(s,0.f);
    STAMP(7);
  } else {
    f32x4 acc[4][7]; float w2f[7];
    const int wm=w>>1, wn=w&1;
    if(tid<8) ldsw<unsigned>(L_CNT+tid*4,0u);
    const rsrc_t rsW=mkrsrc(ws,W_END);
    const int i=4*(wg&7)+w, h=lane>>5, c=lane&31;
    u32x4 gt0,gt1,o3,o2a,o2b; unsigned o1,v1[6],bb[7],ww[7];
    const i32x4 srdO=mksrd(wout,156);
    if(wg<16){
      const i32x4 srdC=mksrd(wc,3*17576*2), srdG=mksrd(g_gtab.v,2048);
      if(wg<8){
        const unsigned so=(lane>>2)*1352+(lane&3)*16;
        dma(L_FA+w*2048,i<26?so:OOB,srdC,35152+i*52); dma(L_FA+w*2048+1024,(i<26&&lane<40)?so:OOB,srdC,35152+16*1352+i*52);
        ld4(o3,104+(lane>>4)*16,srdO,0); ld4(o2a,52+h*16,srdO,0); ld4(o2b,84+h*16,srdO,0);
      } else {
        #pragma unroll
        for(int k=0;k<6;k++) ld1(v1[k],lane*4+k*256,srdC,(i<26?i:0)*1352);
      }
      ld4(gt0,lane*32,srdG,0); ld4(gt1,lane*32+16,srdG,0);
    }
    { const i32x4 sb=mksrd(b1,DIN*2), sw=mksrd(w2,DIN*2); const unsigned vo=(lane&15)*2;
      #pragma unroll
      for(int n=0;n<7;n++){ const unsigned o=(nt*208+(n<6?wn*112+n*16:96))*2; ldh(bb[n],vo,sb,o); ldh(ww[n],vo,sw,o); } }
    ldh(o1,c<26?c*2:OOB,srdO,0);
    unsigned aA0,aA1,aB0,aB1,aB6;
    { int row=wm*64+(lane&15); int s=(row>>1)&7;
      aA0=row*128+(((4*wn+(lane>>4))^s)*16); aA1=aA0^64;
      int trrow=4*(lane>>4)+((lane&15)>>2);
      unsigned b=L_W+trrow*WPITCH+(lane&3)*8;
      aB0=b+32*wn*WPITCH+wn*224; aB1=b+32*(wn^1)*WPITCH+wn*224; aB6=b+32*wn*WPITCH+192; }
    asm volatile("":"+v"(aA0),"+v"(aA1),"+v"(aB0),"+v"(aB1),"+v"(aB6));
    #pragma unroll
    for(int n=0;n<7;n++){ acc[2][n]=f32x4{0,0,0,0}; acc[3][n]=f32x4{0,0,0,0}; pin(acc[2][n]); pin(acc[3][n]); }
    wait_ep();
    if(wg<8){ waitv<15>(); lds_flag(L_FL+w*4,ep); }
    auto init_acc=[&](){
    waitv<0>();
    pin(o1); ldsw<float>(L_O1+c*4,bf2f(o1));
    #pragma unroll
    for(int n=0;n<7;n++){ pin(bb[n]); pin(ww[n]); float b=bf2f(bb[n]); w2f[n]=bf2f(ww[n]);
      float b0=(n<6||wn==0)?b:0.f, b1v=(n<6||wn==1)?b:0.f;
      acc[0][n]=f32x4{b0,b0,b0,b0}; acc[1][n]=f32x4{b1v,b1v,b1v,b1v};
      asm volatile("":"+v"(acc[0][n]),"+v"(acc[1][n]),"+v"(w2f[n])); } };
    init_acc();
    if(wg<16){
      const unsigned dm=L_FD+w*4352;
      pin(gt0); pin(gt1);
      if(wg<8){
        pin(o3); pin(o2a); pin(o2b);
        { bool to=lds_flags(L_FL,7,ep,lane); DBG(0,to); }
        { const int g=lane>>4, qq=(lane&15)>>2; int r0=8*g+qq, r1=r0+4; r0=r0<26?r0:25; r1=r1<26?r1:25;
          const unsigned t0=L_F3+r0*1352+(lane&3)*8+w*32, t1=L_F3+r1*1352+(lane&3)*8+w*32;
          f32x16 d={};
          #pragma unroll
          for(int k=0;k<11;k++){ f32x4 z={0,0,0,0}; d[k]=MFMA16(BC8(o3),cat(ldT(t0+k*128),ldT(t1+k*128)),z)[0]; }
          #pragma unroll
          for(int rd=0;rd<3;rd++){
            float pa=(g&1)?d[4*rd+1]:d[4*rd], pb=(g&1)?d[4*rd+3]:d[4*rd+2]; pin(pa); pin(pb); float pv=(g&2)?pb:pa;
            int e=16*(w+4*(rd*4+g))+(lane&15);
            int gi=e/26; unsigned hi=cvtpk(pv,0.f), lo=cvtpk(pv-bflo(hi),0.f);
            unsigned a=L_FP+gi*64+(e-gi*26)*2; a=e<676? a : L_FP+62;
            ldsw<u16>(a,(u16)hi); ldsw<u16>(a+2048,(u16)lo);
          } }
        lds_flag(L_FL+32+w*4,ep);
        f32x16 D={}, N={};
        { const int rq=8*h+((lane&15)>>2); const unsigned tc=((lane>>4)&1)*32+(lane&3)*8, ta=L_FA+w*2048+rq*64+tc;
          bf16x8 af0=cat(ldT(ta),ldT(ta+256)), af1=cat(ldT(ta+1024),ldT(ta+1280));
          N=MFMA32(af0,BC8(o2a),N); N=MFMA32(af1,BC8(o2b),N);
          const unsigned p0=L_FP+rq*64+tc, p2=L_FP+min(rq+16,25)*64+tc, p3=L_FP+min(rq+20,25)*64+tc;
          { bool to=lds_flags(L_FL+32,3,ep,lane); DBG(1,to); }
          D=MFMA32(af0,cat(ldT(p0),ldT(p0+256)),D);           D=MFMA32(af1,cat(ldT(p2),ldT(p3)),D);
          D=MFMA32(af0,cat(ldT(p0+2048),ldT(p0+2304)),D);     D=MFMA32(af1,cat(ldT(p2+2048),ldT(p3+2048)),D); }
        #pragma unroll
        for(int r=0;r<16;r++) ldsw<float>(dm+(((r&3)+8*(r>>2)+4*h)*33+c)*4,c==26?N[r]:D[r]);
      } else {
        #pragma unroll
        for(int k=0;k<6;k++){ pin(v1[k]); int e=lane+64*k; int a=e/13,b=2*(e-13*a); ldsw<float>(dm+(a*33+b)*4,bflo(v1[k])); ldsw<float>(dm+(a*33+b+1)*4,bfhi(v1[k])); }
      }
      ldsw<float>(dm+1056*4,0.f);
      { const int hh=lane&1, s=lane>>1; float v[8];
        #pragma unroll
        for(int k=0;k<8;k++){ unsigned o=k<4?gt0[k]:gt1[k-4]; v[k]=ldsr<float>(dm+(o&0xffff))+ldsr<float>(dm+(o>>16)); }
        u32x4 o; o[0]=cvtpk(v[0],v[1]); o[1]=cvtpk(v[2],v[3]); o[2]=cvtpk(v[4],v[5]); o[3]=cvtpk(v[6],v[7]);
        if(i>=26) o=u32x4{0,0,0,0};
        int fq= s<23? 2*s+(wg<8?1:0) : 23+s;
        bool on= s<23 || (s<25&&wg<8);
        __builtin_amdgcn_raw_buffer_store_b128(o,rsW,on?W_FRAG+fq*1024+(hh*32+i)*16:OOB,0,SC1);
      }
    }
    bf16x8 A0[4]; s16x4 B0[14];
    barl();
    STAMP(1);
    if(wg<8 && tid<12) ldsw<unsigned>(L_FL+tid*4,~ep);
    #pragma unroll
    for(int k=0;k<2;k++) B0[k]=ldT(aB0+k*16*WPITCH);
    #pragma unroll
    for(int m=0;m<4;m++) A0[m]=ldsr<bf16x8>(aA0+m*2048);
    #pragma unroll
    for(int k=2;k<12;k++) B0[k]=ldT(aB0+(k>>1)*32+(k&1)*16*WPITCH);
    B0[12]=ldT(aB6); B0[13]=ldT(aB6+16*WPITCH);
    gemm_stage<0,false>(acc,A0,B0,aA0,aA1,aB0,aB1,aB6);
    waitv<0>();
    STAMP(7);
    #pragma nounroll
    for(int it=0;it<8;it++){
      gemm_stage<1,false>(acc,A0,B0,aA0,aA1,aB0,aB1,aB6);
      gemm_stage<2,false>(acc,A0,B0,aA0,aA1,aB0,aB1,aB6);
      gemm_stage<0,false>(acc,A0,B0,aA0,aA1,aB0,aB1,aB6);
    }
    gemm_stage<1,true>(acc,A0,B0,aA0,aA1,aB0,aB1,aB6);
    asm volatile("s_nop 7\n\ts_nop 7":::"memory"); SB0();
    __builtin_amdgcn_s_setprio(CPRIO);
    STAMP(2);
    { unsigned d[24];
      #pragma unroll
      for(int n=0;n<6;n++){ d[4*n]=cvtpk(acc[2][n][0],acc[2][n][1]); d[4*n+1]=cvtpk(acc[2][n][2],acc[2][n][3]); d[4*n+2]=cvtpk(acc[3][n][0],acc[3][n][1]); d[4*n+3]=cvtpk(acc[3][n][2],acc[3][n][3]); }
      const unsigned so=W_XBUF+((wg^1)*4+w)*10240;
      #pragma unroll
      for(int k=0;k<8;k++) st4_sc1(u32x4{d[3*k],d[3*k+1],d[3*k+2],ep},lane*16,srdS,so+k*1024);
      #pragma unroll
      for(int m=0;m<2;m++) st4_sc1(u32x4{cvtpk(acc[2+m][6][0],acc[2+m][6][1]),cvtpk(acc[2+m][6][2],acc[2+m][6][3]),0u,ep},lane*16,srdS,so+(8+m)*1024);
    }
    ldsw<f32x4>(L_B6+w*1024+lane*16, wn?acc[0][6]:acc[1][6]);
    lds_arrive(L_CNT+8+wm*4);
    STAMP(3);
    u32x4 pr[10];
    auto fetch=[&](){
      const unsigned so=W_XBUF+(wg*4+w)*10240;
      #pragma unroll
      for(int k=0;k<8;k++) ld4_sc1(pr[k],lane*16,srdS,so+k*1024);
      ld4_sc1(pr[8],lane*16,srdS,so+(8+wn)*1024);
      ld4_sc1(pr[9],lane*16,srdS,W_XBUF+(wg*4+(w^1))*10240+(8+wn)*1024); };
    { unsigned tg, spins=0;
      const unsigned po=W_XBUF+(wg*4+(w^(lane&1)))*10240+(8+wn)*1024+12;
      while(true){ ld1_sc1(tg,po,srdS,0); waitv<0>(); pin(tg); if(__all(tg==ep)||++spins>SPIN_CAP) break; __builtin_amdgcn_s_sleep(8); } }
    fetch();
    { bool to=lds_wait(L_CNT+8+wm*4,2); DBG(2,to); }
    f32x4 o6=ldsr<f32x4>(L_B6+(w^1)*1024+lane*16);
    { unsigned spins=0;
      while(true){ bool ok=true;
        waitv<0>();
        #pragma unroll
        for(int k=0;k<10;k++){ pin(pr[k]); ok&=pr[k][3]==ep; }
        if(__all(ok)||++spins>SPIN_CAP) break;
        __builtin_amdgcn_s_sleep(4); fetch(); }
      DBG(6,spins>SPIN_CAP); }
    STAMP(4);
    float rs[8]={0,0,0,0,0,0,0,0};
    #pragma unroll
    for(int n=0;n<6;n++){
      #pragma unroll
      for(int m=0;m<2;m++){ const int e=4*n+2*m; const unsigned p0=pr[e/3][e%3], p1=pr[(e+1)/3][(e+1)%3];
        rs[4*m+0]+=gelu_w(acc[m][n][0]+bflo(p0),w2f[n]); rs[4*m+1]+=gelu_w(acc[m][n][1]+bfhi(p0),w2f[n]);
        rs[4*m+2]+=gelu_w(acc[m][n][2]+bflo(p1),w2f[n]); rs[4*m+3]+=gelu_w(acc[m][n][3]+bfhi(p1),w2f[n]);
      } }
    { f32x4 k6=wn?acc[1][6]:acc[0][6];
      float g0=gelu_w(k6[0]+o6[0]+bflo(pr[8][0])+bflo(pr[9][0]),w2f[6]), g1=gelu_w(k6[1]+o6[1]+bfhi(pr[8][0])+bfhi(pr[9][0]),w2f[6]);
      float g2=gelu_w(k6[2]+o6[2]+bflo(pr[8][1])+bflo(pr[9][1]),w2f[6]), g3=gelu_w(k6[3]+o6[3]+bfhi(pr[8][1])+bfhi(pr[9][1]),w2f[6]);
      rs[0]+=wn?0.f:g0; rs[1]+=wn?0.f:g1; rs[2]+=wn?0.f:g2; rs[3]+=wn?0.f:g3; rs[4]+=wn?g0:0.f; rs[5]+=wn?g1:0.f; rs[6]+=wn?g2:0.f; rs[7]+=wn?g3:0.f; }
    #pragma unroll
    for(int k=0;k<8;k++) rs[k]=sum16(rs[k]);
    { const int cc=lane&15; float v=rs[0];
      #pragma unroll
      for(int k=1;k<8;k++) v=(cc==k)?rs[k]:v;
      int owner=mt*32+wm*16+(2*ks+(cc>>2))*4+(lane>>4);
      if(cc<8) __hip_atomic_store((GAS u64*)(ws+W_GRAN+owner*1024+(nt*2+wn)*32+(cc&3)*8),((u64)ep<<32)|__float_as_uint(v),RLX_AGENT);
    }
    STAMP(5);
    if(wg==0 && w==0){
      unsigned spins=0; const GAS unsigned* sp=(const GAS unsigned*)(ws+W_STARTED)+lane;
      while(true){ bool ok=true;
        #pragma unroll
        for(int k=0;k<4;k++) ok&=__hip_atomic_load(sp+64*k,RLX_AGENT)==ep;
        if(__all(ok)||++spins>SPIN_CAP) break; __builtin_amdgcn_s_sleep(1); }
      if(lane==0) __hip_atomic_store((GAS unsigned*)(ws+W_EPOCH),ep,RLX_AGENT);
    }
  }
}
extern "C" void kernel_launch(void* const* d_in,const int* in_sizes,int n_in,void* d_out,int out_size,void* d_ws,size_t ws_size,hipStream_t stream){
  (void)hipFuncSetAttribute((const void*)k_main,hipFuncAttributeMaxDynamicSharedMemorySize,L_TOTAL);
  k_main<<<256,512,L_TOTAL,stream>>>((const int*)d_in[0],(const u16*)d_in[1],(const u16*)d_in[2],(const u16*)d_in[3],(const u16*)d_in[4],(const u16*)d_in[5],(const u16*)d_in[6],(const u16*)d_in[7],(u16*)d_out,(char*)d_ws);
}
```

```cpp
#include <hip/hip_runtime.h>
#include <hip/hip_bf16.h>
typedef unsigned short u16;
typedef unsigned long long u64;
typedef int i32x4 __attribute__((ext_vector_type(4)));
typedef unsigned u32x4 __attribute__((ext_vector_type(4)));
typedef unsigned u32x2 __attribute__((ext_vector_type(2)));
typedef short s16x4 __attribute__((ext_vector_type(4)));
typedef __bf16 bf16x8 __attribute__((ext_vector_type(8)));
typedef __bf16 bf16x2 __attribute__((ext_vector_type(2)));
typedef float f32x2 __attribute__((ext_vector_type(2)));
typedef float f32x4 __attribute__((ext_vector_type(4)));
typedef float f32x16 __attribute__((ext_vector_type(16)));
#define DI __device__ __forceinline__
#define LAS __attribute__((address_space(3)))
#define GAS __attribute__((address_space(1)))
constexpr int NB=1024,NF=26,ND=128,DIN=3328,ROWB=DIN*2;
constexpr int ASLOT=16384, WSLOT=26624, WPITCH=416, L_W=3*ASLOT;
constexpr int L_A2=2*ASLOT, L_W2=L_W+2*WSLOT;
constexpr int L_CW=L_W+3*WSLOT, L_IDX=L_CW+26624, L_MISC=L_IDX+7168, L_TOTAL=163840;
constexpr int L_X0=L_CW, L_CNT=L_MISC, L_FL=L_MISC+64, L_O1=L_MISC+128, L_PFD=L_MISC+768;
constexpr int L_B6=ASLOT;
constexpr int L_F3=L_W2, L_FA=L_W2+35840, L_FP=L_FA+8192, L_FD=L_W2;
static_assert(L_FP+2048<=L_IDX && L_MISC+1024==L_TOTAL,"lds map");
constexpr unsigned W_EPOCH=0, W_STARTED=256, W_FFLAG=2048, W_XFLAG=4096, W_FRAG=4096+65536, W_GRAN=W_FRAG+49152, W_XBUF=W_GRAN+262144, W_END=W_XBUF+256*4*7168;
constexpr unsigned CODE_BYTES=22528;
constexpr unsigned SPIN_CAP=1u<<18, OOB=0x7ffffff0u;

DI float bf2f(unsigned x){ return __uint_as_float(x<<16); }
DI float bflo(unsigned x){ return __uint_as_float(x<<16); }
DI float bfhi(unsigned x){ return __uint_as_float(x&0xffff0000u); }
DI unsigned cvtpk(float lo,float hi){ f32x2 v={lo,hi}; bf16x2 b=__builtin_convertvector(v,bf16x2); return __builtin_bit_cast(unsigned,b); }
DI void dma(unsigned m0v, unsigned voff, i32x4 srd, unsigned soff){
  asm volatile("s_mov_b32 m0, %0\n\ts_nop 0\n\tbuffer_load_dwordx4 %1, %2, %3 offen lds" :: "s"(m0v), "v"(voff), "s"(srd), "s"(soff) : "memory");
}
DI void dma_sc1(unsigned m0v, unsigned voff, i32x4 srd, unsigned soff){
  asm volatile("s_mov_b32 m0, %0\n\ts_nop 0\n\tbuffer_load_dwordx4 %1, %2, %3 offen sc1 lds" :: "s"(m0v), "v"(voff), "s"(srd), "s"(soff) : "memory");
}
DI void dma_lo32(unsigned m0v, unsigned voff, i32x4 srd, unsigned soff){
  asm volatile("s_mov_b32 m0, %0\n\ts_mov_b64 exec, %4\n\tbuffer_load_dwordx4 %1, %2, %3 offen lds\n\ts_mov_b64 exec, -1" :: "s"(m0v), "v"(voff), "s"(srd), "s"(soff), "s"(0xFFFFFFFFull) : "memory");
}
constexpr u64 MASK52=0x000FFFFFFFFFFFFFull;
template<int I> DI void dmaW(unsigned m0v, unsigned voff, i32x4 srd, unsigned so){
  unsigned t;
  asm volatile("s_add_u32 m0, %1, %6\n\ts_add_u32 %0, %4, %7\n\ts_mov_b64 exec, %5\n\tbuffer_load_dwordx4 %2, %3, %0 offen lds\n\ts_mov_b64 exec, -1"
    : "=&s"(t) : "s"(m0v), "v"(voff), "s"(srd), "s"(so), "s"(MASK52), "n"(832*I), "n"((8*(I>>1)+2*(I&1))*ROWB) : "memory","scc");
}
DI void dmaW8(unsigned m0v, unsigned voff, i32x4 srd, unsigned so){
  dmaW<0>(m0v,voff,srd,so); dmaW<1>(m0v,voff,srd,so); dmaW<2>(m0v,voff,srd,so); dmaW<3>(m0v,voff,srd,so);
  dmaW<4>(m0v,voff,srd,so); dmaW<5>(m0v,voff,srd,so); dmaW<6>(m0v,voff,srd,so); dmaW<7>(m0v,voff,srd,so);
}
DI void pfload(unsigned voff, i32x4 srd, unsigned soff){
  asm volatile("s_mov_b32 m0, %3\n\ts_nop 0\n\tbuffer_load_dword %0, %1, %2 offen lds" :: "v"(voff), "s"(srd), "s"(soff), "n"(L_PFD) : "memory"); }
DI void ld4_sc1(u32x4& d,unsigned voff,i32x4 srd,unsigned soff){ asm volatile("buffer_load_dwordx4 %0, %1, %2, %3 offen sc1":"=v"(d):"v"(voff),"s"(srd),"s"(soff):"memory"); }
DI void st4_sc1(u32x4 d,unsigned voff,i32x4 srd,unsigned soff){ asm volatile("buffer_store_dwordx4 %0, %1, %2, %3 offen sc1\n\ts_nop 1"::"v"(d),"v"(voff),"s"(srd),"s"(soff):"memory"); }
DI void ld1_sc1(unsigned& d,unsigned voff,i32x4 srd,unsigned soff){ asm volatile("buffer_load_dword %0, %1, %2, %3 offen sc1":"=v"(d):"v"(voff),"s"(srd),"s"(soff):"memory"); }
DI void ld4(u32x4& d,unsigned voff,i32x4 srd,unsigned soff){ asm volatile("buffer_load_dwordx4 %0, %1, %2, %3 offen":"=v"(d):"v"(voff),"s"(srd),"s"(soff):"memory"); }
DI void ldh(unsigned& d,unsigned voff,i32x4 srd,unsigned soff){ asm volatile("buffer_load_ushort %0, %1, %2, %3 offen":"=v"(d):"v"(voff),"s"(srd),"s"(soff):"memory"); }
DI void ld1(unsigned& d,unsigned voff,i32x4 srd,unsigned soff){ asm volatile("buffer_load_dword %0, %1, %2, %3 offen":"=v"(d):"v"(voff),"s"(srd),"s"(soff):"memory"); }
template<class T> DI void pin(T& x){ asm volatile("":"+v"(x)); }
template<int N> DI void waitv(){ asm volatile("s_waitcnt vmcnt(%0)"::"n"(N):"memory"); }
DI void bar(){ asm volatile("s_barrier":::"memory"); }
DI void barl(){ asm volatile("s_waitcnt lgkmcnt(0)\n\ts_barrier":::"memory"); }
DI i32x4 mksrd(const void* p, unsigned bytes){
  u64 a=(u64)p; i32x4 s;
  s[0]=__builtin_amdgcn_readfirstlane((unsigned)a); s[1]=__builtin_amdgcn_readfirstlane((unsigned)(a>>32)&0xffff); s[2]=bytes; s[3]=0x20000; return s;
}
#define RLX_AGENT __ATOMIC_RELAXED, __HIP_MEMORY_SCOPE_AGENT
#define SB0() __builtin_amdgcn_sched_barrier(0)
template<class T> DI T ldsr(unsigned a){ return *(const LAS T*)(size_t)a; }
template<class T> DI void ldsw(unsigned a,T v){ *(LAS T*)(size_t)a=v; }
DI s16x4 ldT(unsigned a){ return __builtin_amdgcn_ds_read_tr16_b64_v4i16((LAS s16x4*)(size_t)a); }
DI bf16x8 cat(s16x4 lo,s16x4 hi){ return __builtin_bit_cast(bf16x8,__builtin_shufflevector(lo,hi,0,1,2,3,4,5,6,7)); }
#define MFMA16(a,b,c) __builtin_amdgcn_mfma_f32_16x16x32_bf16(a,b,c,0,0,0)
#define MFMA32(a,b,c) __builtin_amdgcn_mfma_f32_32x32x16_bf16(a,b,c,0,0,0)
#define BC8(x) __builtin_bit_cast(bf16x8,x)
template<int CTRL> DI float dppget(float v){ return __int_as_float(__builtin_amdgcn_update_dpp(0,__float_as_int(v),CTRL,0xf,0xf,true)); }
template<int CTRL> DI float dppadd(float v){ return v+__int_as_float(__builtin_amdgcn_update_dpp(0,__float_as_int(v),CTRL,0xf,0xf,true)); }
DI float sum16(float v){ v=dppadd<0xB1>(v); v=dppadd<0x4E>(v); v=dppadd<0x141>(v); v=dppadd<0x140>(v); return v; }
DI float rl(float v,int l){ return __int_as_float(__builtin_amdgcn_readlane(__float_as_int(v),l)); }
DI unsigned lds_peek(unsigned addr){ unsigned v,r;
  asm volatile("ds_read_b32 %0, %2\n\ts_waitcnt lgkmcnt(0)\n\tv_readfirstlane_b32 %1, %0":"=&v"(v),"=s"(r):"v"(addr):"memory"); return r; }
DI void lds_arrive(unsigned cnt){ asm volatile("s_waitcnt lgkmcnt(0)\n\ts_mov_b64 exec, 1\n\tds_add_u32 %0, %1\n\ts_mov_b64 exec, -1"::"v"(cnt),"v"(1u):"memory"); }
DI bool lds_wait(unsigned cnt,unsigned n){ unsigned spins=0; while(lds_peek(cnt)<n && ++spins<=SPIN_CAP) __builtin_amdgcn_s_sleep(0); return spins>SPIN_CAP; }
DI void lds_flag(unsigned addr){ asm volatile("s_waitcnt lgkmcnt(0)":::"memory"); ldsw<unsigned>(addr,1u); }
DI bool lds_flags(unsigned addr,unsigned mask,int lane){ unsigned spins=0, a=addr+(lane&mask)*4, v;
  while(true){ asm volatile("ds_read_b32 %0, %1\n\ts_waitcnt lgkmcnt(0)":"=v"(v):"v"(a):"memory"); if(__all(v==1u)||++spins>SPIN_CAP) break; }
  return spins>SPIN_CAP; }
DI void mfma16_acc(f32x4& c,bf16x8 a,bf16x8 b){ asm volatile("v_mfma_f32_16x16x32_bf16 %0, %1, %2, %0":"+v"(c):"v"(a),"v"(b)); }

template<int S,bool LAST> DI void gemm_stage(f32x4 (&acc)[4][7],bf16x8 (&A0)[4],s16x4 (&B0)[14],unsigned aA0,unsigned aA1,unsigned aB0,unsigned aB1,unsigned aB6){
  constexpr int SN=(S+1)%3, HI=16*WPITCH;
  bf16x8 A1[4]; s16x4 B1[12];
  #pragma unroll
  for(int i=0;i<28;i++){ const int n=i>>2,m=(n&1)?3-(i&3):(i&3);
    mfma16_acc(acc[m][n],A0[m],cat(B0[2*n],B0[2*n+1])); SB0();
    if(i<2) B1[i]=ldT(aB1+S*WSLOT+(i&1)*HI);
    else if(i<6) A1[i-2]=ldsr<bf16x8>(aA1+S*ASLOT+(i-2)*2048);
    else if(i<16){ const int k=i-4; B1[k]=ldT(aB1+S*WSLOT+(k>>1)*32+(k&1)*HI); }
    SB0();
    if(i==26){ barl(); SB0(); }
  }
  #pragma unroll
  for(int i=0;i<24;i++){ const int n=i>>2,m=(n&1)?3-(i&3):(i&3);
    mfma16_acc(acc[m][n],A1[m],cat(B1[2*n],B1[2*n+1])); SB0();
    if(!LAST){
      if(i<2) B0[i]=ldT(aB0+SN*WSLOT+(i&1)*HI);
      else if(i<6) A0[i-2]=ldsr<bf16x8>(aA0+SN*ASLOT+(i-2)*2048);
      else if(i<16){ const int k=i-4; B0[k]=ldT(aB0+SN*WSLOT+(k>>1)*32+(k&1)*HI); }
      else if(i<18) B0[i-4]=ldT(aB6+SN*WSLOT+(i&1)*HI);
    }
    SB0();
  }
}

template<int T> struct Slot { static constexpr int u=T>>1, a=T<182?u%13:0, b=T<182?(u%13+u/13)%13:0; };
DI void mfma32_acc(f32x16& c,u32x4 a,u32x4 b){ asm volatile("v_mfma_f32_32x32x16_bf16 %0, %1, %2, %0":"+v"(c):"v"(a),"v"(b)); }
DI void zq(unsigned& o0,unsigned& o1,float a0,f32x2 b0,float a1,f32x2 b1){
  float t0,t1,t2,t3;
  asm volatile("v_mul_f32_e32 %2, %6, %7\n\tv_mul_f32_e32 %3, %6, %8\n\tv_mul_f32_e32 %4, %9, %10\n\tv_mul_f32_e32 %5, %9, %11\n\tv_cvt_pk_bf16_f32 %0, %2, %3\n\tv_cvt_pk_bf16_f32 %1, %4, %5"
    :"=&v"(o0),"=&v"(o1),"=&v"(t0),"=&v"(t1),"=&v"(t2),"=&v"(t3):"v"(a0),"v"(b0[0]),"v"(b0[1]),"v"(a1),"v"(b1[0]),"v"(b1[1]));
}
DI void zl(unsigned& o0,unsigned& o1,float a,float b,float c,float d){ asm volatile("v_cvt_pk_bf16_f32 %0, %2, %3\n\tv_cvt_pk_bf16_f32 %1, %4, %5":"=&v"(o0),"=&v"(o1):"v"(a),"v"(b),"v"(c),"v"(d)); }
template<int S,int H> DI void zhalf(const f32x2 (&R)[13],u32x4& z){
  unsigned o0,o1;
  if constexpr(S<23){ using P=Slot<8*S+4*H>; using Q=Slot<8*S+4*H+2>; zq(o0,o1,R[P::a][0],R[P::b],R[Q::a][0],R[Q::b]); }
  else if constexpr(S==23) zl(o0,o1,R[4*H][0],R[4*H+1][0],R[4*H+2][0],R[4*H+3][0]);
  else if constexpr(S==24){ if constexpr(H==0) zl(o0,o1,R[8][0],R[9][0],R[10][0],R[11][0]); else zl(o0,o1,R[12][0],R[12][0],R[12][0],R[12][0]); }
  if constexpr(S<25){ z[2*H]=o0; z[2*H+1]=o1; }
}
__host__ __device__ constexpr unsigned wfrag_addr(int q){ return q<26? L_W2+q*1024 : q<42? L_A2+(q-26)*1024 : L_IDX+(q-42)*1024; }
DI void mfma32_0(f32x16& c,u32x4 a,u32x4 b){ asm volatile("v_mfma_f32_32x32x16_bf16 %0, %1, %2, 0":"=&v"(c):"v"(a),"v"(b)); }
DI float fma_s(float a,float b,float c){ float r; asm("v_fma_f32 %0, %1, %2, %3":"=v"(r):"v"(a),"v"(b),"v"(c)); return r; }
struct Cin { f32x2 R0[13],R1[13]; f32x16 x0,v0,x1,v1; u32x4 z0[2],z1[2],wa[2],wb[2]; unsigned la,lb; };
template<int Q> DI u32x4 wfrag(const Cin& c){ if constexpr(Q>=26&&Q<42) return ldsr<u32x4>(c.la+(wfrag_addr(Q)-L_A2)); else return ldsr<u32x4>(c.lb+(wfrag_addr(Q)-L_W2)); }
template<int S> DI void cin_step(Cin& c){
  if constexpr(S==0) mfma32_0(c.x0,c.wa[0],c.z0[0]); else if constexpr(S<23) mfma32_acc(c.x0,c.wa[S&1],c.z0[S&1]);
  SB0(); zhalf<S+1,0>(c.R0,c.z0[(S+1)&1]); SB0();
  mfma32_acc(c.v0,c.wb[S&1],c.z0[S&1]);
  SB0(); zhalf<S+1,1>(c.R0,c.z0[(S+1)&1]); SB0();
  if constexpr(S==0) mfma32_0(c.x1,c.wa[0],c.z1[0]); else if constexpr(S<23) mfma32_acc(c.x1,c.wa[S&1],c.z1[S&1]);
  SB0(); if constexpr(S+2<23) c.wa[S&1]=wfrag<2*S+4>(c);
  zhalf<S+1,0>(c.R1,c.z1[(S+1)&1]); SB0();
  if constexpr(S==24) asm volatile("s_nop 0");
  mfma32_acc(c.v1,c.wb[S&1],c.z1[S&1]);
  SB0(); if constexpr(S+2<23) c.wb[S&1]=wfrag<2*S+5>(c); else if constexpr(S+2<25) c.wb[S&1]=wfrag<23+S+2>(c);
  zhalf<S+1,1>(c.R1,c.z1[(S+1)&1]); SB0();
}
template<int S,int E> DI void cin_run(Cin& c){ if constexpr(S<E){ cin_step<S>(c); cin_run<S+1,E>(c); } }

struct GTab { u16 v[64*16]; };
constexpr GTab make_gtab(){ GTab t{};
  for(int c=0;c<64;c++) for(int k=0;k<8;k++){ int hh=c&1,s=c>>1,o0=1056,o1=1056;
    if(s<23){ int tt=8*s+k,u=tt>>1,dl=u/13,a=u%13,b=(a+dl)%13+13*(k&1),pa=a+13*hh,pb=(b+13*hh)%26;
      if(tt<182 && !(hh&&(k&1)&&dl==0)){ o0=pa*33+pb; if(pa!=pb) o1=pb*33+pa; } }
    else if(s<25){ int a=8*(s-23)+k; if(a<13) o0=(a+13*hh)*33+26; }
    t.v[c*16+2*k]=(u16)(o0*4); t.v[c*16+2*k+1]=(u16)(o1*4); }
  return t; }
__device__ const GTab g_gtab=make_gtab();
DI float gelu_acc(float acc,float x,float iw){
  const float e=__builtin_amdgcn_exp2f(x*(-2.302208198f-0.1029432397f*x*x));
  return __builtin_fmaf(x,__builtin_amdgcn_rcpf(__builtin_fmaf(e,iw,iw)),acc);
}

struct Ldr { i32x4 srdT,srdW; unsigned wv,wl,al,t4,asw0,asw1,so,sa,sw,st; u64 perm; };
constexpr int fperm(int f,int rot){ const int lo=f<9? f/3*3 : 9, n=f<9? 3 : 4; return lo+(f-lo+rot)%n; }
constexpr u64 fpack(int lo,int hi,int rot){ u64 p=0; for(int f=lo;f<hi;f++) p|=(u64)fperm(f,rot)<<(4*f); return p; }
template<int P> DI void dmaA(Ldr& L,const u32x4& ro){ dma(L.sa+L.al+P*1024,ro[P]+((P&1)?L.asw1:L.asw0),L.srdT,(L.st&1)*128); }
DI void ldr_issue(Ldr& L){
  const unsigned f=(unsigned)(L.perm>>((L.st>>1)*4))&15u;
  u32x4 ro=ldsr<u32x4>(L.t4+f*512);
  dmaW8(L.sw+L.wl,L.wv,L.srdW,L.so+(2*f+(L.st&1))*64*ROWB);
  dmaA<0>(L,ro); dmaA<1>(L,ro); dmaA<2>(L,ro); dmaA<3>(L,ro);
  L.sw=(L.sa==2*ASLOT)?0:L.sw+WSLOT; L.sa=(L.sa==2*ASLOT)?0:L.sa+ASLOT; L.st+=1;
}

__global__ __launch_bounds__(512,2) void k_main(const int* __restrict__ idx,const u16* __restrict__ table,const u16* __restrict__ wc,const u16* __restrict__ wout,
    const u16* __restrict__ w1,const u16* __restrict__ b1,const u16* __restrict__ w2,const u16* __restrict__ bout,u16* __restrict__ out,char* ws){
  extern __shared__ __attribute__((aligned(1024))) char lds[];
  const int tid=threadIdx.x, lane=tid&63, w=__builtin_amdgcn_readfirstlane(tid>>6);
  const int wg=blockIdx.x, xcd=wg&7, j=wg>>3, ks=xcd&1, mt=j&7, nl=j>>3, nt=(xcd>>1)*4+nl;
  if(w==0 && j>=2 && j<8){
    u64 pc; asm volatile("s_getpc_b64 %0":"=s"(pc));
    pfload(lane<32? (unsigned)(((j-2)*32+lane)*128) : OOB,mksrd((const void*)(pc&~255ull),CODE_BYTES),0);
  }
  asm volatile(""::"s"(idx),"s"(table),"s"(wc),"s"(wout),"s"(w1),"s"(b1),"s"(w2),"s"(bout),"s"(out),"s"(ws));
  if(wg<8){
    const i32x4 srdC=mksrd(wc,3*17576*2);
    if(tid<12) ldsw<unsigned>(L_FL+tid*4,0u);
    #pragma unroll
    for(int k=0;k<5;k++) if(k<4||w<3) dma(L_F3+(w+8*k)*1024,lane*16,srdC,2*35152+(w+8*k)*1024);
    barl();
  }
  unsigned ep;
  unsigned ep_raw;
  asm volatile("s_load_dword %0, %1, 0x0":"=s"(ep_raw):"s"(ws):"memory");
  auto wait_ep=[&](){ asm volatile("s_waitcnt lgkmcnt(0)\n\ts_add_u32 %0, %1, 1":"=s"(ep):"s"(ep_raw):"memory","scc"); };
  const i32x4 srdS=mksrd(ws,W_END);
  if(__builtin_expect(w>=4,1)){
    const int q=w-4;
    Ldr L; L.srdT=mksrd(table,100000u*256u); L.srdW=mksrd(w1,(unsigned)DIN*ROWB);
    const i32x4 srdI=mksrd(idx,NB*NF*4);
    const int r8=lane>>3, c8=lane&7, h=lane>>5;
    const unsigned f0=(mt+nl)%3;
    u32x2 iv[4]; unsigned i0[4], ix;
    const unsigned irow=((mt*128+((32*q)^(32*ks))+r8)*NF+ks*13)*4;
    #pragma unroll
    for(int p=0;p<4;p++) asm volatile("buffer_load_dword %0, %1, %2, %4 offen offset:%3":"=v"(i0[p]):"v"(irow),"s"(srdI),"n"(p*8*NF*4),"s"(f0*4):"memory");
    #pragma unroll
    for(int p=0;p<4;p++) asm volatile("buffer_load_dwordx2 %0, %1, %2, 0 offen offset:%3":"=v"(iv[p]):"v"(irow+8*c8),"s"(srdI),"n"(p*8*NF*4):"memory");
    ld1(ix,(lane<26?lane:0)*4,srdI,(wg*4+q)*NF*4);
    L.wv=(lane>=26?ROWB+(lane-26)*16:lane*16)+nt*WPITCH;
    L.so=(ks*1664+32*(q>>1)+4*(q&1))*ROWB;
    L.wl=L_W+q*16*WPITCH; L.al=q*4096;
    dmaW8(L.wl,L.wv,L.srdW,L.so+f0*128*ROWB);
    L.asw0=(c8^((r8>>1)&7))*16; L.asw1=L.asw0^64;
    { const unsigned r4=(mt+nl)&3;
      L.perm=(f0==0? fpack(0,9,0) : f0==1? fpack(0,9,1) : fpack(0,9,2)) | (r4==0? fpack(9,13,0) : r4==1? fpack(9,13,1) : r4==2? fpack(9,13,2) : fpack(9,13,3)); }
    waitv<13>();
    #pragma unroll
    for(int p=0;p<4;p++) pin(i0[p]);
    #pragma unroll
    for(int p=0;p<4;p++){ i0[p]=(i0[p]<<8)+((p&1)?L.asw1:L.asw0); dma(L.al+p*1024,i0[p],L.srdT,0); }
    waitv<12>();
    #pragma unroll
    for(int p=0;p<4;p++) pin(iv[p]);
    pin(ix);
    wait_ep();
    if(wg<8) lds_flag(L_FL+w*4);
    dmaW8(WSLOT+L.wl,L.wv,L.srdW,L.so+(2*f0+1)*64*ROWB);
    #pragma unroll
    for(int p=0;p<4;p++) dma(ASLOT+L.al+p*1024,i0[p],L.srdT,128);
    if(c8<7){
      #pragma unroll
      for(int p=0;p<4;p++){ ldsw<unsigned>(L_IDX+((2*c8)*128+q*32+r8*4+p)*4,iv[p][0]<<8); ldsw<unsigned>(L_IDX+((2*c8+1)*128+q*32+r8*4+p)*4,iv[p][1]<<8); }
    }
    L.t4=L_IDX+(q*8+r8)*16; L.sa=2*ASLOT; L.sw=2*WSLOT; L.st=2;
    waitv<12>();
    barl();
    __builtin_amdgcn_s_setprio(1);
    if(tid==256) __hip_atomic_store((GAS unsigned*)(ws+W_STARTED)+wg,ep,RLX_AGENT);
    #pragma nounroll
    for(int f=1;f<12;f++){
      ldr_issue(L); waitv<12>(); bar();
      if(f==2 && wg<16 && tid==256) __hip_atomic_store((GAS unsigned*)(ws+W_FFLAG)+wg,ep,RLX_AGENT);
      ldr_issue(L); waitv<12>(); bar(); }
    unsigned fl=ep; if(q==0) ld1_sc1(fl,(lane&15)*4,srdS,W_FFLAG);
    #pragma unroll
    for(int i=0;i<7;i++){ unsigned a=(unsigned)__builtin_amdgcn_ds_bpermute((4*i+(lane>>4))*4,(int)ix)*256u+(lane&15)*16;
      if(i<6) dma(L_X0+q*6656+i*1024,a,L.srdT,0); else dma_lo32(L_X0+q*6656+i*1024,a,L.srdT,0); }
    ldr_issue(L); waitv<12>(); pin(fl);
    { unsigned spins=0; while(!__all(fl==ep) && ++spins<=SPIN_CAP){ __builtin_amdgcn_s_sleep(16); ld1_sc1(fl,(lane&15)*4,srdS,W_FFLAG); waitv<0>(); pin(fl); }
      if(spins>SPIN_CAP) ldsw<float>(L_O1,__uint_as_float(0x7fc00000u)); }
    bar();
    ldr_issue(L); waitv<12>(); bar();
    auto wdma=[&](int i){ const int qq=4*i+q; unsigned a=qq*1024+(qq<26? L_W2 : L_A2-26*1024); a=qq<42? a : qq*1024+(L_IDX-42*1024); dma_sc1(a,lane*16,srdS,W_FRAG+qq*1024); };
    #pragma unroll
    for(int i=0;i<6;i++) wdma(i);
    waitv<6>();
    bar();
    #pragma unroll
    for(int i=6;i<12;i++) wdma(i);
    Cin c; c.la=L_A2+lane*16; c.lb=L_W2+lane*16; pin(c.la); pin(c.lb);
    unsigned raw[26], xa=L_X0+q*6656+(lane&31)*4+h*13*256, xc=L_X0+q*6656+(lane&31)*4+(1-h)*13*256, oa=L_O1+h*16; pin(xa); pin(xc);
    #pragma unroll
    for(int i=0;i<13;i++){ raw[i]=ldsr<unsigned>(xa+i*256); raw[13+i]=ldsr<unsigned>(xc+i*256); }
    const unsigned bo=bout[0];
    waitv<0>();
    bar();
    __builtin_amdgcn_s_setprio(0);
    float s0=0.f,s1=0.f,s2=0.f,s3=0.f; u32x2 gr; const unsigned go=(lane&31)*32;
    #pragma nounroll
    for(int g=0;g<2;g++){
      c.wa[0]=wfrag<0>(c); c.wb[0]=wfrag<1>(c); c.wa[1]=wfrag<2>(c); c.wb[1]=wfrag<3>(c);
      #pragma unroll
      for(int k=0;k<4;k++){ f32x4 o=ldsr<f32x4>(oa+k*32); pin(oa); f32x4 p=ldsr<f32x4>(oa+k*32);
        #pragma unroll
        for(int e=0;e<4;e++){ c.v0[4*k+e]=o[e]; c.v1[4*k+e]=p[e]; } }
      #pragma unroll
      for(int i=0;i<13;i++){ c.R0[i][0]=bflo(raw[i]); c.R1[i][0]=bfhi(raw[i]); c.R0[i][1]=bflo(raw[13+i]); c.R1[i][1]=bfhi(raw[13+i]); pin(c.R0[i]); pin(c.R1[i]); }
      #pragma unroll
      for(int i=0;i<13;i++){ raw[i]=ldsr<unsigned>(xa+128+i*256); raw[13+i]=ldsr<unsigned>(xc+128+i*256); }
      zhalf<0,0>(c.R0,c.z0[0]); zhalf<0,1>(c.R0,c.z0[0]); zhalf<0,0>(c.R1,c.z1[0]); zhalf<0,1>(c.R1,c.z1[0]);
      pin(c.v0); pin(c.v1); SB0();
      cin_run<0,18>(c);
      asm volatile("buffer_load_dwordx2 %0, %1, %2, %3 offen sc1":"=v"(gr):"v"(g?go:OOB),"s"(srdS),"s"(W_GRAN+wg*1024+q*8):"memory"); SB0();
      cin_run<18,25>(c);
      waitv<0>(); pin(gr);
      asm volatile("s_nop 10":"+v"(c.x0),"+v"(c.v0));
      #pragma unroll
      for(int r=0;r<16;r+=2){ s0=fma_s(c.x0[r],c.v0[r],s0); s1=fma_s(c.x0[r+1],c.v0[r+1],s1); }
      asm volatile("":"+v"(c.x1),"+v"(c.v1),"+v"(s0),"+v"(s1));
      #pragma unroll
      for(int r=0;r<16;r+=2){ s2=fma_s(c.x1[r],c.v1[r],s2); s3=fma_s(c.x1[r+1],c.v1[r+1],s3); }
    }
    unsigned spins=0;
    while(!__all(gr[1]==ep) && ++spins<=SPIN_CAP){
      asm volatile("buffer_load_dwordx2 %0, %1, %2, %3 offen sc1\n\ts_waitcnt vmcnt(0)":"=&v"(gr):"v"(go),"s"(srdS),"s"(W_GRAN+wg*1024+q*8):"memory");
      if(spins>1) __builtin_amdgcn_s_sleep(4); }

    float s=sum16(((s0+s1)+(s2+s3))+(lane<32?__uint_as_float(gr[0]):0.f)); s=(rl(s,0)+rl(s,16))+(rl(s,32)+rl(s,48))+bf2f(bo);
    if(spins>SPIN_CAP) s=__uint_as_float(0x7fc00000u);
    if(lane==0) out[wg*4+q]=(u16)cvtpk(s,0.f);
  } else {
    f32x4 acc[4][7]; float w2f[7];
    const int wm=w>>1, wn=w&1;
    if(tid<2) ldsw<unsigned>(L_CNT+tid*4,0u);
    const int i=4*(wg&7)+w, h=lane>>5, c=lane&31;
    u32x4 gt0,gt1,o3,o2a,o2b; unsigned o1,v1[6],bb[7],ww[7];
    const i32x4 srdO=mksrd(wout,156);
    if(wg<16){
      const i32x4 srdC=mksrd(wc,3*17576*2), srdG=mksrd(g_gtab.v,2048);
      if(wg<8){
        const unsigned so=(lane>>2)*1352+(lane&3)*16;
        dma(L_FA+w*2048,i<26?so:OOB,srdC,35152+i*52); dma(L_FA+w*2048+1024,(i<26&&lane<40)?so:OOB,srdC,35152+16*1352+i*52);
        ld4(o3,104+(lane>>4)*16,srdO,0); ld4(o2a,52+h*16,srdO,0); ld4(o2b,84+h*16,srdO,0);
      } else {
        #pragma unroll
        for(int k=0;k<6;k++) ld1(v1[k],lane*4+k*256,srdC,(i<26?i:0)*1352);
      }
      ld4(gt0,lane*32,srdG,0); ld4(gt1,lane*32+16,srdG,0);
    }
    { const i32x4 sb=mksrd(b1,DIN*2), sw=mksrd(w2,DIN*2); const unsigned vo=(lane&15)*2;
      #pragma unroll
      for(int n=0;n<7;n++){ const unsigned o=(nt*208+(n<6?wn*112+n*16:96))*2; ldh(bb[n],vo,sb,o); ldh(ww[n],vo,sw,o); } }
    ldh(o1,c<26?c*2:OOB,srdO,0);
    unsigned aA0,aA1,aB0,aB1,aB6;
    { int row=wm*64+(lane&15); int s=(row>>1)&7;
      aA0=row*128+(((4*wn+(lane>>4))^s)*16); aA1=aA0^64;
      int trrow=4*(lane>>4)+((lane&15)>>2);
      unsigned b=L_W+trrow*WPITCH+(lane&3)*8;
      aB0=b+32*wn*WPITCH+wn*224; aB1=b+32*(wn^1)*WPITCH+wn*224; aB6=b+32*wn*WPITCH+192; }
    asm volatile("":"+v"(aA0),"+v"(aA1),"+v"(aB0),"+v"(aB1),"+v"(aB6));
    #pragma unroll
    for(int n=0;n<7;n++){ acc[2][n]=f32x4{0,0,0,0}; acc[3][n]=f32x4{0,0,0,0}; pin(acc[2][n]); pin(acc[3][n]); }
    bool bad=false;
    if(wg<16){
      const unsigned dm=L_FD+w*4352;
      if(wg<8){
        const int g=lane>>4, qq=(lane&15)>>2, rq=8*h+qq;
        unsigned t0=L_F3+min(8*g+qq,25)*1352+(lane&3)*8+w*32, t1=L_F3+min(8*g+qq+4,25)*1352+(lane&3)*8+w*32, pa3[3];
        #pragma unroll
        for(int rd=0;rd<3;rd++){ const int e=16*(w+4*(rd*4+g))+(lane&15), gi=e/26;
          pa3[rd]= e<676? L_FP+gi*64+(e-gi*26)*2 : L_FP+62; pin(pa3[rd]); }
        unsigned tc=((lane>>4)&1)*32+(lane&3)*8, ta=L_FA+w*2048+rq*64+tc, p0=L_FP+rq*64+tc, p2=L_FP+min(rq+16,25)*64+tc, p3=L_FP+min(rq+20,25)*64+tc;
        pin(t0); pin(t1); pin(ta); pin(p0); pin(p2); pin(p3);
        waitv<15>(); lds_flag(L_FL+w*4);
        pin(o3); pin(o2a); pin(o2b);
        f32x16 D={}, N={};
        const bf16x8 af0=cat(ldT(ta),ldT(ta+256)), af1=cat(ldT(ta+1024),ldT(ta+1280));
        N=MFMA32(af0,BC8(o2a),N); N=MFMA32(af1,BC8(o2b),N);
        bad|=lds_flags(L_FL,7,lane);
        f32x16 d={};
        #pragma unroll
        for(int k=0;k<11;k++){ f32x4 z={0,0,0,0}; d[k]=MFMA16(BC8(o3),cat(ldT(t0+k*128),ldT(t1+k*128)),z)[0]; }
        #pragma unroll
        for(int rd=0;rd<3;rd++){
          float pa=(g&1)?d[4*rd+1]:d[4*rd], pb=(g&1)?d[4*rd+3]:d[4*rd+2]; pin(pa); pin(pb);
          ldsw<u16>(pa3[rd],(u16)cvtpk((g&2)?pb:pa,0.f)); }
        lds_flag(L_FL+32+w*4);
        bad|=lds_flags(L_FL+32,3,lane);
        D=MFMA32(af0,cat(ldT(p0),ldT(p0+256)),D); D=MFMA32(af1,cat(ldT(p2),ldT(p3)),D);
        #pragma unroll
        for(int r=0;r<16;r++) ldsw<float>(dm+(((r&3)+8*(r>>2)+4*h)*33+c)*4,c==26?N[r]:D[r]);
      } else {
        unsigned da[6];
        #pragma unroll
        for(int k=0;k<6;k++){ const int e=lane+64*k, a=e/13; da[k]=dm+(a*33+2*(e-13*a))*4; pin(da[k]); }
        waitv<15>();
        #pragma unroll
        for(int k=0;k<6;k++){ pin(v1[k]); ldsw<float>(da[k],bflo(v1[k])); ldsw<float>(da[k]+4,bfhi(v1[k])); }
      }
      ldsw<float>(dm+1056*4,0.f);
      pin(gt0); pin(gt1);
      { const int hh=lane&1, s=lane>>1; float v[8];
        #pragma unroll
        for(int k=0;k<8;k++){ unsigned o=k<4?gt0[k]:gt1[k-4]; v[k]=ldsr<float>(dm+(o&0xffff))+ldsr<float>(dm+(o>>16)); }
        u32x4 o; o[0]=cvtpk(v[0],v[1]); o[1]=cvtpk(v[2],v[3]); o[2]=cvtpk(v[4],v[5]); o[3]=cvtpk(v[6],v[7]);
        if(i>=26) o=u32x4{0,0,0,0};
        if(bad) o=u32x4{~0u,~0u,~0u,~0u};
        int fq= s<23? 2*s+(wg<8?1:0) : 23+s;
        bool on= s<23 || (s<25&&wg<8);
        st4_sc1(o,on?W_FRAG+fq*1024+(hh*32+i)*16:OOB,srdS,0);
      }
      waitv<1>();
    } else {
      if(w==2){
        const unsigned o= lane==0? W_XBUF+wg*28672 : lane==1? W_XBUF+(wg^1)*28672 : lane==2? W_GRAN+mt*32768 : lane==3? W_FRAG : lane==4? W_XFLAG+wg*256 : OOB;
        asm volatile("s_mov_b32 m0, %2\n\ts_nop 0\n\tbuffer_load_dword %0, %1, 0 offen sc1 lds" :: "v"(o), "s"(srdS), "n"(L_PFD) : "memory"); waitv<1>(); }
      else waitv<0>(); }
    pin(o1); ldsw<float>(L_O1+c*4,bf2f(o1));
    #pragma unroll
    for(int n=0;n<7;n++){ pin(bb[n]); pin(ww[n]); float b=bf2f(bb[n]); w2f[n]=__builtin_amdgcn_fmed3f(__builtin_amdgcn_rcpf(bf2f(ww[n])),-1.7e38f,1.7e38f);
      float b0=(n<6||wn==0)?b:0.f, b1v=(n<6||wn==1)?b:0.f;
      acc[0][n]=f32x4{b0,b0,b0,b0}; acc[1][n]=f32x4{b1v,b1v,b1v,b1v};
      asm volatile("":"+v"(acc[0][n]),"+v"(acc[1][n]),"+v"(w2f[n])); }
    wait_ep();
    bf16x8 A0[4]; s16x4 B0[14];
    barl();
    #pragma unroll
    for(int k=0;k<2;k++) B0[k]=ldT(aB0+k*16*WPITCH);
    #pragma unroll
    for(int m=0;m<4;m++) A0[m]=ldsr<bf16x8>(aA0+m*2048);
    #pragma unroll
    for(int k=2;k<12;k++) B0[k]=ldT(aB0+(k>>1)*32+(k&1)*16*WPITCH);
    B0[12]=ldT(aB6); B0[13]=ldT(aB6+16*WPITCH);
    gemm_stage<0,false>(acc,A0,B0,aA0,aA1,aB0,aB1,aB6);
    waitv<0>();
    #pragma nounroll
    for(int it=0;it<8;it++){
      gemm_stage<1,false>(acc,A0,B0,aA0,aA1,aB0,aB1,aB6);
      gemm_stage<2,false>(acc,A0,B0,aA0,aA1,aB0,aB1,aB6);
      gemm_stage<0,false>(acc,A0,B0,aA0,aA1,aB0,aB1,aB6);
    }
    gemm_stage<1,true>(acc,A0,B0,aA0,aA1,aB0,aB1,aB6);
    asm volatile("s_nop 7\n\ts_nop 7":::"memory"); SB0();
    #pragma unroll
    for(int n=0;n<7;n++) st4_sc1(u32x4{cvtpk(acc[2][n][0],acc[2][n][1]),cvtpk(acc[2][n][2],acc[2][n][3]),cvtpk(acc[3][n][0],acc[3][n][1]),cvtpk(acc[3][n][2],acc[3][n][3])},
                                 lane*16,srdS,W_XBUF+((wg^1)*4+w)*7168+n*1024);
    ldsw<f32x4>(L_B6+w*1024+lane*16, wn?acc[0][6]:acc[1][6]);
    lds_arrive(L_CNT+wm*4);
    u32x4 sel0, sel1;
    { const unsigned one=((lane>>4)==((lane&15)>>2))? (0x3f80u<<(16*(lane&1))) : 0u, lo=(lane&2)?0u:one, hi=(lane&2)?one:0u;
      sel0=u32x4{lo,hi,0u,0u}; sel1=u32x4{0u,0u,lo,hi}; }
    bad|=lds_wait(L_CNT+wm*4,2);
    f32x4 k6=(wn?acc[1][6]:acc[0][6])+ldsr<f32x4>(L_B6+(w^1)*1024+lane*16);
    u32x4 sel6=wn?sel1:sel0;
    waitv<0>();
    if(lane==0) __hip_atomic_store((GAS unsigned*)(ws+W_XFLAG+(wg*4+w)*64),ep,RLX_AGENT);
    { unsigned tg, spins=0; const unsigned po=W_XFLAG+((wg^1)*4+(w^(lane&1)))*64;
      while(true){ ld1_sc1(tg,po,srdS,0); waitv<0>(); pin(tg); if(__all(tg==ep)||++spins>SPIN_CAP) break; __builtin_amdgcn_s_sleep(4); }
      bad|=spins>SPIN_CAP; }
    u32x4 pr[8];
    #pragma unroll
    for(int n=0;n<7;n++) ld4_sc1(pr[n],lane*16,srdS,W_XBUF+(wg*4+w)*7168+n*1024);
    ld4_sc1(pr[7],lane*16,srdS,W_XBUF+(wg*4+(w^1))*7168+6*1024);
    asm volatile("s_nop 1\n\ts_waitcnt vmcnt(0)":"+v"(sel0),"+v"(sel1),"+v"(sel6),"+v"(k6)::"memory");
    #pragma unroll
    for(int n=0;n<6;n++){ pin(pr[n]); mfma16_acc(acc[0][n],BC8(sel0),BC8(pr[n])); mfma16_acc(acc[1][n],BC8(sel1),BC8(pr[n])); }
    pin(pr[6]); pin(pr[7]); mfma16_acc(k6,BC8(sel6),BC8(pr[6])); mfma16_acc(k6,BC8(sel6),BC8(pr[7]));
    asm volatile("s_nop 7":::"memory"); SB0();
    float rs[8]={0,0,0,0,0,0,0,0};
    #pragma unroll
    for(int n=0;n<6;n++){
      #pragma unroll
      for(int e=0;e<8;e++) rs[e]=gelu_acc(rs[e],acc[e>>2][n][e&3],w2f[n]); }
    { float g0=gelu_acc(0.f,k6[0],w2f[6]), g1=gelu_acc(0.f,k6[1],w2f[6]), g2=gelu_acc(0.f,k6[2],w2f[6]), g3=gelu_acc(0.f,k6[3],w2f[6]);
      rs[0]+=wn?0.f:g0; rs[1]+=wn?0.f:g1; rs[2]+=wn?0.f:g2; rs[3]+=wn?0.f:g3; rs[4]+=wn?g0:0.f; rs[5]+=wn?g1:0.f; rs[6]+=wn?g2:0.f; rs[7]+=wn?g3:0.f; }
    {
      const int cc=lane&15; const bool b0=cc&1, b1=cc&2, b2=cc&4; float a4[4],a2[2];
      #pragma unroll
      for(int k=0;k<4;k++) a4[k]=(b0?rs[2*k+1]:rs[2*k])+dppget<0xB1>(b0?rs[2*k]:rs[2*k+1]);
      #pragma unroll
      for(int k=0;k<2;k++) a2[k]=(b1?a4[2*k+1]:a4[2*k])+dppget<0x4E>(b1?a4[2*k]:a4[2*k+1]);
      float v=(b2?a2[1]:a2[0])+dppget<0x124>(b2?a2[0]:a2[1]);
      v+=dppget<0x128>(v);
      if(bad) v=__uint_as_float(0x7fc00000u);
      int owner=mt*32+wm*16+(2*ks+(cc>>2))*4+(lane>>4);
      if(cc<8) __hip_atomic_store((GAS u64*)(ws+W_GRAN+owner*1024+(nt*2+wn)*32+(cc&3)*8),((u64)ep<<32)|__float_as_uint(v),RLX_AGENT);
    }
    if(wg==0 && w==0){
      unsigned spins=0; const GAS unsigned* sp=(const GAS unsigned*)(ws+W_STARTED)+lane;
      while(true){ bool ok=true;
        #pragma unroll
        for(int k=0;k<4;k++) ok&=__hip_atomic_load(sp+64*k,RLX_AGENT)==ep;
        if(__all(ok)||++spins>SPIN_CAP) break; __builtin_amdgcn_s_sleep(1); }
      if(lane==0) __hip_atomic_store((GAS unsigned*)(ws+W_EPOCH),ep,RLX_AGENT);
    }
  }
}
extern "C" void kernel_launch(void* const* d_in,const int* in_sizes,int n_in,void* d_out,int out_size,void* d_ws,size_t ws_size,hipStream_t stream){
  (void)hipFuncSetAttribute((const void*)k_main,hipFuncAttributeMaxDynamicSharedMemorySize,L_TOTAL);
  k_main<<<256,512,L_TOTAL,stream>>>((const int*)d_in[0],(const u16*)d_in[1],(const u16*)d_in[2],(const u16*)d_in[3],(const u16*)d_in[4],(const u16*)d_in[5],(const u16*)d_in[6],(const u16*)d_in[7],(u16*)d_out,(char*)d_ws);
}
```
